```python
import torch

def _softmax(x, axis=-1):
    return torch.softmax(x, dim=axis)
def _gelu(x):
    return torch.nn.functional.gelu(x, approximate="tanh")
def _silu(x):
    return torch.nn.functional.silu(x)
def _sigmoid(x):
    return torch.sigmoid(x)
def _layernorm(x, axis=-1, eps=1e-5):
    m = x.mean(dim=axis, keepdim=True)
    v = x.var(dim=axis, keepdim=True, unbiased=False)
    return (x - m) / torch.sqrt(v + eps)
def _scatter_add(tgt, idx, src, axis=0):
    return tgt.clone().index_add_(axis, idx, src)
def _segment_sum(src, seg, n):
    out = torch.zeros((n,) + src.shape[1:], dtype=src.dtype)
    return out.index_add_(0, seg, src)
def _pad(x, pw):
    flat = tuple(v for pair in reversed(pw) for v in pair)
    return torch.nn.functional.pad(x, flat)

kernel_name = 'recurrent_scan__sliding_local_dilated__dense_activation_squared_relu__prompt'

def setup_inputs():

    _w = (lambda a: (lambda t: t.to(torch.bfloat16) if t.is_floating_point() else t)(torch.as_tensor(a)))
    def _A_setup():
        B, S, D = 1, 2048, 640
        return {
            "x": _w((lambda *s: torch.randn(*s, dtype=torch.bfloat16) * 0.05)(B, S, D)),
            "wqkv": _w((lambda *s: torch.randn(*s, dtype=torch.bfloat16) * 0.05)(D, 3 * D)),
            "wo": _w((lambda *s: torch.randn(*s, dtype=torch.bfloat16) * 0.05)(D, D)),
        }
    _A_in = _A_setup()

    def _B_setup():
        B, S, D, F = 1, 2048, 640, 1536
        return {
            "x": _w((lambda *s: torch.randn(*s, dtype=torch.bfloat16) * 0.05)(B, S, D)),
            "w1": _w((lambda *s: torch.randn(*s, dtype=torch.bfloat16) * 0.05)(D, F)),
            "b1": _w((lambda *s: torch.randn(*s, dtype=torch.bfloat16) * 0.4)(F)),
            "w2": _w((lambda *s: torch.randn(*s, dtype=torch.bfloat16) * 0.05)(F, D)),
            "b2": _w((lambda *s: torch.randn(*s, dtype=torch.bfloat16) * 1.6)(D)),
        }
    _B_in = _B_setup()

    return {
        "A_x": _A_in["x"],
        "A_wqkv": _A_in["wqkv"],
        "A_wo": _A_in["wo"],
        "B_x": _B_in["x"],
        "B_w1": _B_in["w1"],
        "B_b1": _B_in["b1"],
        "B_w2": _B_in["w2"],
        "B_b2": _B_in["b2"],
    }

def reference(A_x, A_wqkv, A_wo, B_x, B_w1, B_b1, B_w2, B_b2):
    def _A_ref(x, wqkv, wo):
        B, S, D = x.shape
        H = 32; Dh = D // H
        W, dil = 8, 2
        qkv = (lambda x, *d: x.permute(*d))((x @ wqkv).reshape(B, S, 3, H, Dh), 2, 0, 3, 1, 4)
        q, k, v = qkv[0], qkv[1], qkv[2]
        scale = Dh ** -0.5
        pos = torch.arange(S)
        off = torch.arange(W) * dil
        idx = pos[:, None] - off[None, :]
        mask = idx >= 0
        idx_c = torch.where(mask, idx, 0).reshape(-1)
        kg = k[:, :, idx_c, :].reshape(B, H, S, W, Dh)
        vg = v[:, :, idx_c, :].reshape(B, H, S, W, Dh)
        logits = torch.einsum("bhsd,bhswd->bhsw", q, kg) * scale
        logits = torch.where(mask[None, None, :, :], logits, -1e9)
        attn = _softmax(logits, axis=-1)
        o = torch.einsum("bhsw,bhswd->bhsd", attn, vg)
        return (lambda x, *d: x.permute(*d))(o, 0, 2, 1, 3).reshape(B, S, D) @ wo

    def _B_ref(x, w1, b1, w2, b2):
        h = torch.einsum("bsd,df->bsf", x, w1) + b1
        zero = torch.zeros_like(h)
        r = torch.where(h > zero, h, zero)
        h = r * r
        out = torch.einsum("bsf,fd->bsd", h, w2) + b2
        return out

    x = A_x
    xn = _layernorm(x, axis=-1)
    s = _A_ref(xn, A_wqkv, A_wo)
    g = _silu(_B_ref(xn, B_w1, B_b1, B_w2, B_b2))
    return x + s * g
```

```cpp
#include <hip/hip_runtime.h>
#include <hip/hip_bf16.h>
typedef short bf16x8 __attribute__((ext_vector_type(8)));
typedef float f32x4 __attribute__((ext_vector_type(4)));
typedef unsigned short u16;

__device__ __forceinline__ float bf2f(u16 b){ return __builtin_bit_cast(float, (unsigned)b << 16); }
__device__ __forceinline__ u16 f2bf(float f){ __hip_bfloat16 h = __float2bfloat16(f); return __builtin_bit_cast(u16, h); }

constexpr int S=2048, D=640, F=1536, H=32, DH=20;

__global__ void ln_k(const u16* x, u16* xn){
  int row = blockIdx.x; int t = threadIdx.x;
  float v[10]; float s=0;
  for(int i=0;i<10;++i){ v[i]=bf2f(x[row*D + t + 64*i]); s+=v[i]; }
  for(int o=32;o;o>>=1) s+=__shfl_xor(s,o);
  float m = s/D; float q=0;
  for(int i=0;i<10;++i){ float d=v[i]-m; q+=d*d; }
  for(int o=32;o;o>>=1) q+=__shfl_xor(q,o);
  float r = 1.0f/sqrtf(q/D+1e-5f);
  for(int i=0;i<10;++i) xn[row*D+t+64*i]=f2bf((v[i]-m)*r);
}

template<int MODE>
__global__ __launch_bounds__(256) void gemm_k(const u16* A, int lda, const u16* B, int ldb, int K,
   u16* C, int ldc, const u16* bias, const u16* A2, int lda2, const u16* B2, int K2, const u16* x){
  __shared__ u16 As[64][40];
  __shared__ u16 Bs[64][40];
  int t=threadIdx.x, w=t>>6, l=t&63;
  int m0=blockIdx.y*64, n0=blockIdx.x*64;
  int wr=w>>1, wc=w&1;
  f32x4 acc[2][2][2]={};
  for(int pass=0; pass<(MODE==2?2:1); ++pass){
    const u16* Ap = pass? A2:A; const u16* Bp = pass? B2:B; int Kp = pass?K2:K; int la = pass?lda2:lda;
    for(int k0=0;k0<Kp;k0+=32){
      __syncthreads();
      { int r=t>>2, c=(t&3)*8; *(bf16x8*)&As[r][c] = *(const bf16x8*)&Ap[(size_t)(m0+r)*la + k0 + c]; }
      { int kk=t>>3, c=(t&7)*8; bf16x8 v=*(const bf16x8*)&Bp[(size_t)(k0+kk)*ldb + n0 + c];
        for(int j=0;j<8;++j) Bs[c+j][kk]=(u16)v[j]; }
      __syncthreads();
      bf16x8 a[2], b[2];
      for(int i=0;i<2;++i){ a[i]=*(const bf16x8*)&As[wr*32+i*16+(l&15)][(l>>4)*8]; b[i]=*(const bf16x8*)&Bs[wc*32+i*16+(l&15)][(l>>4)*8]; }
      if(pass==0){ for(int i=0;i<2;++i)for(int j=0;j<2;++j) acc[0][i][j]=__builtin_amdgcn_mfma_f32_16x16x32_bf16(a[i],b[j],acc[0][i][j],0,0,0);}
      else { for(int i=0;i<2;++i)for(int j=0;j<2;++j) acc[1][i][j]=__builtin_amdgcn_mfma_f32_16x16x32_bf16(a[i],b[j],acc[1][i][j],0,0,0);}
    }
  }
  for(int i=0;i<2;++i)for(int j=0;j<2;++j)for(int r=0;r<4;++r){
    int row=m0+wr*32+i*16+(l>>4)*4+r, col=n0+wc*32+j*16+(l&15);
    float v=acc[0][i][j][r];
    if(MODE==0){ C[(size_t)row*ldc+col]=f2bf(v); }
    else if(MODE==1){ v+=bf2f(bias[col]); v=v>0?v:0; C[(size_t)row*ldc+col]=f2bf(v*v); }
    else { float g=acc[1][i][j][r]+bf2f(bias[col]); g=g/(1.0f+__expf(-g)); C[(size_t)row*ldc+col]=f2bf(bf2f(x[(size_t)row*D+col])+v*g); }
  }
}

__global__ void attn_k(const u16* qkv, u16* o){
  int idx=blockIdx.x*blockDim.x+threadIdx.x; int s=idx/H, h=idx%H;
  float q[DH]; for(int d=0;d<DH;++d) q[d]=bf2f(qkv[(size_t)s*1920+h*DH+d]);
  float lg[8]; float mx=-1e30f;
  for(int w=0;w<8;++w){ int p=s-2*w; float a=-1e9f; if(p>=0){ a=0; for(int d=0;d<DH;++d) a+=q[d]*bf2f(qkv[(size_t)p*1920+640+h*DH+d]); a*=0.22360679774997896f;} lg[w]=a; mx=fmaxf(mx,a);}
  float sum=0; for(int w=0;w<8;++w){ lg[w]=__expf(lg[w]-mx); sum+=lg[w]; }
  float inv=1.0f/sum;
  for(int d=0;d<DH;++d){ float a=0; for(int w=0;w<8;++w){ int p=s-2*w; if(p<0)p=0; a+=lg[w]*bf2f(qkv[(size_t)p*1920+1280+h*DH+d]); } o[(size_t)s*D+h*DH+d]=f2bf(a*inv);}
}

extern "C" void kernel_launch(void* const* d_in, const int* in_sizes, int n_in,
                              void* d_out, int out_size, void* d_ws, size_t ws_size,
                              hipStream_t stream) {
  const u16* x=(const u16*)d_in[0]; const u16* wqkv=(const u16*)d_in[1]; const u16* wo=(const u16*)d_in[2];
  const u16* w1=(const u16*)d_in[4]; const u16* b1=(const u16*)d_in[5]; const u16* w2=(const u16*)d_in[6]; const u16* b2=(const u16*)d_in[7];
  u16* out=(u16*)d_out;
  u16* xn=(u16*)d_ws; u16* qkv=xn+S*D; u16* h2=qkv+S*1920; u16* o=h2+S*F;
  ln_k<<<S,64,0,stream>>>(x,xn);
  gemm_k<0><<<dim3(1920/64,S/64),256,0,stream>>>(xn,D,wqkv,1920,D,qkv,1920,nullptr,nullptr,0,nullptr,0,nullptr);
  gemm_k<1><<<dim3(F/64,S/64),256,0,stream>>>(xn,D,w1,F,D,h2,F,b1,nullptr,0,nullptr,0,nullptr);
  attn_k<<<S*H/256,256,0,stream>>>(qkv,o);
  gemm_k<2><<<dim3(D/64,S/64),256,0,stream>>>(o,D,wo,D,D,out,D,b2,h2,F,w2,F,x);
}
```

```cpp
#include <hip/hip_runtime.h>
#include <hip/hip_bf16.h>

typedef unsigned short u16;
typedef short bf16x8 __attribute__((ext_vector_type(8)));
typedef short bf16x4 __attribute__((ext_vector_type(4)));
typedef float f32x4 __attribute__((ext_vector_type(4)));
typedef unsigned v4u __attribute__((ext_vector_type(4)));
typedef unsigned v2u __attribute__((ext_vector_type(2)));
typedef __bf16 bfx2 __attribute__((ext_vector_type(2)));
typedef float f32x2 __attribute__((ext_vector_type(2)));
#define LAS __attribute__((address_space(3)))
#define GAS __attribute__((address_space(1)))
#define DEVI __device__ __forceinline__

constexpr int S = 2048, D = 640, F = 1536;
constexpr size_t OFF_H2 = 65536;
constexpr size_t OFF_O = OFF_H2 + 6291456;
constexpr size_t OFF_WP = OFF_O + 2621440;
constexpr size_t WS_USED = OFF_WP + 2785280;

#define O8 0x3F80, 0x3F80, 0x3F80, 0x3F80, 0x3F80, 0x3F80, 0x3F80, 0x3F80
#define O64 O8, O8, O8, O8, O8, O8, O8, O8
static __device__ const __attribute__((aligned(128))) u16 ONES_ROW[640] = {O64, O64, O64, O64, O64, O64, O64, O64, O64, O64};

DEVI float bfhi(unsigned u) { return __uint_as_float(u & 0xFFFF0000u); }
DEVI float bflo(unsigned u) { return __uint_as_float(u << 16); }
DEVI unsigned pk2(float a, float b) {
  f32x2 v = {a, b};
  bfx2 r = __builtin_convertvector(v, bfx2);
  return __builtin_bit_cast(unsigned, r);
}
DEVI float rbf(float a) { return bflo(pk2(a, a)); }
DEVI f32x4 mfma(v4u a, v4u b, f32x4 c) {
  return __builtin_amdgcn_mfma_f32_16x16x32_bf16(__builtin_bit_cast(bf16x8, a), __builtin_bit_cast(bf16x8, b), c, 0, 0, 0);
}
DEVI v2u trread(const char* p) {
  bf16x4 r = __builtin_amdgcn_ds_read_tr16_b64_v4i16((LAS bf16x4*)p);
  return __builtin_bit_cast(v2u, r);
}
DEVI float xsum16_32(float v) {
  auto a = __builtin_amdgcn_permlane16_swap(__float_as_uint(v), __float_as_uint(v), false, false);
  v = __uint_as_float(a[0]) + __uint_as_float(a[1]);
  auto b = __builtin_amdgcn_permlane32_swap(__float_as_uint(v), __float_as_uint(v), false, false);
  return __uint_as_float(b[0]) + __uint_as_float(b[1]);
}
DEVI float xmax16_32(float v) {
  auto a = __builtin_amdgcn_permlane16_swap(__float_as_uint(v), __float_as_uint(v), false, false);
  v = fmaxf(__uint_as_float(a[0]), __uint_as_float(a[1]));
  auto b = __builtin_amdgcn_permlane32_swap(__float_as_uint(v), __float_as_uint(v), false, false);
  return fmaxf(__uint_as_float(b[0]), __uint_as_float(b[1]));
}
#define WAITV(n) asm volatile("s_waitcnt vmcnt(%0)" ::"i"(n) : "memory")
#define WAITL0() __builtin_amdgcn_s_waitcnt(0xC07F)
#define SB() __builtin_amdgcn_sched_barrier(0)
DEVI void glds16(const void* g, unsigned lds_dst) {
  unsigned keep;
  asm volatile("s_mov_b32 %0, m0\n\ts_mov_b32 m0, %2\n\ts_nop 0\n\tglobal_load_lds_dwordx4 %1, off\n\ts_mov_b32 m0, %0"
               : "=&s"(keep) : "v"(g), "s"(lds_dst) : "memory");
}
DEVI void acc_stats(unsigned u, float& s1, float& s2) {
  const float a = bflo(u), b = bfhi(u);
  asm("v_add_f32 %0, %0, %1" : "+v"(s1) : "v"(a));
  asm("v_fmac_f32 %0, %1, %1" : "+v"(s2) : "v"(a));
  asm("v_add_f32 %0, %0, %1" : "+v"(s1) : "v"(b));
  asm("v_fmac_f32 %0, %1, %1" : "+v"(s2) : "v"(b));
}
DEVI int vrow2row(int v) { return ((v & 1023) << 1) | (v >> 10); }

#define STAMP(k, i)

constexpr int K1_STAGE = 47104, K1_LDS = 3 * K1_STAGE;
constexpr int RS = 528;
constexpr int L_ST = 69632;
constexpr int L_CS = L_ST + 5120;

template <bool QKV>
DEVI void gemm_unit(const u16* __restrict__ x, const u16* __restrict__ W, const u16* __restrict__ b1,
                    char* __restrict__ ws, char* smem, int rt, int ct) {
  constexpr int MI = QKV ? 2 : 5, NI = QKV ? 8 : 3;
  constexpr int NA = QKV ? 16 : 20;
  constexpr int NP = QKV ? 46 : 44;
  constexpr int BOFF = NA * 1024;
  constexpr int LDW = QKV ? 3 * D : F;
  constexpr int ROWS = NA * 8;
  const int tid = threadIdx.x, l = tid & 63;
  const int w = __builtin_amdgcn_readfirstlane(tid >> 6);
  const int wm = QKV ? (w & 3) : (w >> 2), wn = QKV ? (w >> 2) : (w & 3);
  const int m16 = l & 15, kg = l >> 4;
  const unsigned lds0 = (unsigned)(size_t)(LAS char*)smem;
  const int par = QKV ? (rt >= 9 ? 1 : 0) : 0;
  const int tI = QKV ? rt - 9 * par : 0;
  const int v0 = QKV ? par * 1024 + 114 * tI - 7 : 158 * rt;
  const int vlo = QKV ? par * 1024 : 0, vhi = QKV ? par * 1024 + 1023 : 2047;

  const char* gp[6];
#pragma unroll
  for (int i = 0; i < 6; ++i) {
    const int pc = w + 8 * i;
    if (i < 2 || (!QKV && i == 2 && w < 4)) {
      const int j = l >> 3, pos = l & 7;
      const int lr = pc * 8 + j;
      int v = v0 + lr;
      v = v < vlo ? vlo : (v > vhi ? vhi : v);
      const u16* rp = lr == ROWS - 1 ? ONES_ROW : x + (size_t)vrow2row(v) * D;
      gp[i] = (const char*)rp + ((pos ^ ((lr >> 1) & 7)) << 4);
    } else if (QKV) {
      const int pb = pc - NA < 30 ? pc - NA : 29;
      const int e = pb * 64 + l, r = e / 30, s16 = e % 30;
      int T = (s16 >> 1) + 4 * ((r >> 3) & 1);
      T = T >= 15 ? T - 15 : T;
      gp[i] = (const char*)(W + (size_t)r * LDW + (T / 5) * D + ct * 80 + (T % 5) * 16 + (s16 & 1) * 8);
    } else {
      const int j = l >> 3, pos = l & 7;
      const int pb = pc - NA < 24 ? pc - NA : 23;
      const int kb = pb / 3, Pp = pb % 3;
      const int c32 = (pos >> 1) ^ (((j >> 1) & 1) | ((kb & 1) << 1));
      gp[i] = (const char*)(W + (size_t)(kb * 8 + j) * LDW + ct * 192 + Pp * 64) + c32 * 32 + (pos & 1) * 16;
    }
  }
  const int step2 = (!QKV && w < 4) ? 128 : LDW * 128;
  const bool has6 = w + 40 < NP;
  auto issue1 = [&](int i, unsigned lb) {
    if (i < 5 || has6) {
      glds16(gp[i], lb + i * 8192);
      gp[i] += i < 2 ? 128 : (i == 2 ? step2 : LDW * 128);
    }
  };
#pragma unroll
  for (int i = 0; i < 6; ++i) issue1(i, lds0 + w * 1024);
#pragma unroll
  for (int i = 0; i < 6; ++i) issue1(i, lds0 + K1_STAGE + w * 1024);

  v2u bias[3];
  if (!QKV) {
#pragma unroll
    for (int ni = 0; ni < 3; ++ni) bias[ni] = *(const v2u*)(b1 + ct * 192 + wn * 48 + ni * 16 + kg * 4);
  }

  const int fsw = m16 >> 1;
  const int xrow = (wm * (MI * 16) + m16) * 128;
  const int xa0 = xrow + ((kg ^ fsw) << 4), xa1 = xrow + (((4 + kg) ^ fsw) << 4);
  const int xs = QKV ? xrow + (((4 * wn + kg) ^ fsw) << 4) : xrow + (((2 * wn + (kg >> 1)) ^ fsw) << 4) + (kg & 1) * 8;
  int wb[3];
  {
    const int q = (l >> 2) & 3, pp = l & 3;
    if (QKV) {
      const int base = BOFF + (8 * kg + q) * 480 + pp * 8 + wn * 256;
      wb[1] = base - 128 * (kg & 1);
      wb[0] = wn == 0 ? base + 352 * (kg & 1) : wb[1];
      wb[2] = 0;
    } else {
      const int swz = ((q >> 1) & 1) | ((kg & 1) << 1);
#pragma unroll
      for (int ni = 0; ni < 3; ++ni) {
        const int T = wn * 3 + ni;
        wb[ni] = BOFF + (kg * 3 + (T >> 2)) * 1024 + q * 128 + (((T & 3) ^ swz) << 5) + pp * 8;
      }
    }
  }

  f32x4 acc[MI][NI];
#pragma unroll
  for (int a = 0; a < MI; ++a)
#pragma unroll
    for (int b = 0; b < NI; ++b) acc[a][b] = f32x4{0.f, 0.f, 0.f, 0.f};
  float s1[MI], s2[MI];
#pragma unroll
  for (int a = 0; a < MI; ++a) { s1[a] = 0.f; s2[a] = 0.f; }

  v4u xf0[MI], wf0[NI], xf1[MI], wf1[NI];
  v4u sv[MI];
#pragma unroll
  for (int a = 0; a < MI; ++a) { xf1[a] = v4u{0u, 0u, 0u, 0u}; sv[a] = v4u{0u, 0u, 0u, 0u}; }
#pragma unroll
  for (int b = 0; b < NI; ++b) wf1[b] = v4u{0u, 0u, 0u, 0u};

  auto rd = [&](v4u(&xf)[MI], v4u(&wf)[NI], const char* sb, int kk) {
#pragma unroll
    for (int mi = 0; mi < MI; ++mi) xf[mi] = *(const v4u*)(sb + (kk ? xa1 : xa0) + mi * 2048);
#pragma unroll
    for (int ni = 0; ni < NI; ++ni) {
      const char* p = QKV ? sb + wb[ni < 4 ? 0 : 1] + ni * 32 + kk * 15360 : sb + wb[ni] + kk * 12288;
      const v2u a = trread(p), b = trread(p + (QKV ? 1920 : 512));
      wf[ni] = v4u{a.x, a.y, b.x, b.y};
    }
  };
  auto mm = [&](v4u(&xf)[MI], v4u(&wf)[NI], int g) {
    if (QKV) {
#pragma unroll
      for (int ni = (g == 0 ? 0 : g == 1 ? 3 : 5); ni < (g == 0 ? 3 : g == 1 ? 5 : 7); ++ni)
#pragma unroll
        for (int mi = 0; mi < MI; ++mi) acc[mi][ni] = mfma(wf[ni], xf[mi], acc[mi][ni]);
      if (g == 2 && wn == 0) {
#pragma unroll
        for (int mi = 0; mi < MI; ++mi) acc[mi][NI - 1] = mfma(wf[NI - 1], xf[mi], acc[mi][NI - 1]);
      }
    } else {
#pragma unroll
      for (int mi = 0; mi < MI; ++mi) acc[mi][g] = mfma(wf[g], xf[mi], acc[mi][g]);
    }
  };
  auto rdstats = [&](const char* sb) {
#pragma unroll
    for (int mi = 0; mi < MI; ++mi) {
      if (QKV) sv[mi] = *(const v4u*)(sb + xs + mi * 2048);
      else { const v2u t = *(const volatile v2u*)(sb + xs + mi * 2048); sv[mi].x = t.x; sv[mi].y = t.y; }
    }
  };
  auto dostats = [&](int g) {
    if (QKV) {
      if (g < 2) { acc_stats(sv[g].x, s1[g], s2[g]); acc_stats(sv[g].y, s1[g], s2[g]); acc_stats(sv[g].z, s1[g], s2[g]); }
      else { acc_stats(sv[0].w, s1[0], s2[0]); acc_stats(sv[1].w, s1[1], s2[1]); }
    } else {
#pragma unroll
      for (int mi = (g == 0 ? 0 : g == 1 ? 2 : 4); mi < (g == 0 ? 2 : g == 1 ? 4 : 5); ++mi) {
        acc_stats(sv[mi].x, s1[mi], s2[mi]); acc_stats(sv[mi].y, s1[mi], s2[mi]);
      }
    }
  };

  int buf = 0;
  for (int s = 0; s < 10; ++s) {
    if (s < 9) { if (has6) WAITV(6); else WAITV(5); } else WAITV(0);
    WAITL0();
    __builtin_amdgcn_s_barrier();
    if (s == 0) STAMP(0, 1);
    const char* sb = smem + buf * K1_STAGE;
    const unsigned lb = lds0 + (buf == 0 ? 2 : buf - 1) * K1_STAGE + w * 1024;
    const bool more = s < 8;
    rd(xf0, wf0, sb, 0);
    SB();
#pragma unroll
    for (int g = 0; g < 3; ++g) {
      mm(xf1, wf1, g);
      dostats(g);
      SB();
      if (more) issue1(g, lb);
      SB();
    }
    rd(xf1, wf1, sb, 1);
    rdstats(sb);
    SB();
#pragma unroll
    for (int g = 0; g < 3; ++g) {
      mm(xf0, wf0, g);
      SB();
      if (more) issue1(3 + g, lb);
      SB();
    }
    buf = buf == 2 ? 0 : buf + 1;
  }
#pragma unroll
  for (int g = 0; g < 3; ++g) { mm(xf1, wf1, g); dostats(g); }
  STAMP(0, 2);

  __syncthreads();
  float* st = (float*)(smem + L_ST);
  float* csl = (float*)(smem + L_CS);
#pragma unroll
  for (int mi = 0; mi < MI; ++mi) {
    const float a = xsum16_32(s1[mi]), b = xsum16_32(s2[mi]);
    if (kg == 0) *(f32x2*)(st + (wn * 160 + wm * (MI * 16) + mi * 16 + m16) * 2) = f32x2{a, b};
  }
  if (wm == (QKV ? 3 : 1) && m16 == 15) {
#pragma unroll
    for (int ni = 0; ni < NI; ++ni) *(f32x4*)(csl + (wn * NI + ni) * 16 + kg * 4) = acc[MI - 1][ni];
  }
  __syncthreads();
  float mb[MI], inv[MI];
#pragma unroll
  for (int mi = 0; mi < MI; ++mi) {
    const int lr = wm * (MI * 16) + mi * 16 + m16;
    float a = 0.f, b = 0.f;
#pragma unroll
    for (int j = 0; j < (QKV ? 2 : 4); ++j) {
      const f32x2 v = *(const f32x2*)(st + (j * 160 + lr) * 2);
      a += v.x; b += v.y;
    }
    const float mean = a * (1.0f / D);
    float var = b * (1.0f / D) - mean * mean;
    var = var > 0.f ? var : 0.f;
    mb[mi] = rbf(mean);
    inv[mi] = 1.0f / rbf(sqrtf(rbf(rbf(var) + 1e-5f)));
  }
  if (!QKV) {
    f32x4 cs[NI];
#pragma unroll
    for (int ni = 0; ni < NI; ++ni) cs[ni] = *(const f32x4*)(csl + (wn * NI + ni) * 16 + kg * 4);
#pragma unroll
    for (int mi = 0; mi < MI; ++mi) {
      const int lr = wm * 80 + mi * 16 + m16;
      const int v = v0 + lr;
      v2u pk[3];
#pragma unroll
      for (int ni = 0; ni < 3; ++ni) {
        float t[4];
#pragma unroll
        for (int r = 0; r < 4; ++r) t[r] = (acc[mi][ni][r] - mb[mi] * cs[ni][r]) * inv[mi];
        t[0] += bflo(bias[ni].x); t[1] += bfhi(bias[ni].x); t[2] += bflo(bias[ni].y); t[3] += bfhi(bias[ni].y);
#pragma unroll
        for (int r = 0; r < 4; ++r) { const float u = t[r] > 0.f ? t[r] : 0.f; t[r] = u * u; }
        pk[ni] = v2u{pk2(t[0], t[1]), pk2(t[2], t[3])};
      }
      if (lr < 158 && v < S) {
        char* hb = ws + OFF_H2 + ((size_t)(v >> 6) * 48 * 4 + ((v >> 4) & 3)) * 1024 + (kg * 16 + (v & 15)) * 16;
        const int t0 = ct * 6 + ((3 * wn) >> 1);
        if (wn & 1) {
          *(v2u*)(hb + (size_t)t0 * 4096 + 8) = pk[0];
          *(v4u*)(hb + (size_t)(t0 + 1) * 4096) = v4u{pk[1].x, pk[1].y, pk[2].x, pk[2].y};
        } else {
          *(v4u*)(hb + (size_t)t0 * 4096) = v4u{pk[0].x, pk[0].y, pk[1].x, pk[1].y};
          *(v2u*)(hb + (size_t)(t0 + 1) * 4096) = pk[2];
        }
      }
    }
  } else {
#pragma unroll
    for (int ni = 0; ni < NI; ++ni) {
      const f32x4 cs = *(const f32x4*)(csl + (wn * NI + ni) * 16 + kg * 4);
#pragma unroll
      for (int mi = 0; mi < MI; ++mi) {
        float t[4];
#pragma unroll
        for (int r = 0; r < 4; ++r) t[r] = (acc[mi][ni][r] - mb[mi] * cs[r]) * inv[mi];
        *(v2u*)(smem + (wm * 32 + mi * 16 + m16) * RS + (wn * 8 + ni) * 32 + kg * 8) = v2u{pk2(t[0], t[1]), pk2(t[2], t[3])};
      }
    }
    __syncthreads();
    const int h = w & 3, u0 = (w >> 2) * 4;
    const int q4 = (l >> 2) & 3, pp = l & 3;
    const float sc = 0.22360679774997896f * 1.4426950408889634f;
    const f32x4 z = {0.f, 0.f, 0.f, 0.f};
    const int chi = 20 * ct + 5 * h;
#pragma unroll
    for (int un = 0; un < 4; ++un) {
      const int u = u0 + un;
      const int rq = 16 * u + m16;
      const int ra = u > 0 ? rq - 16 : rq;
      const char* qp = smem + rq * RS + h * 40;
      v2u q0 = *(const v2u*)(qp + kg * 8), q1 = *(const v2u*)(qp + 32);
      if (kg != 0) q1 = v2u{0u, 0u};
      const v4u qf = {q0.x, q0.y, q1.x, q1.y};
      const char* kb = qp + 160;
      const char* ka = smem + ra * RS + h * 40 + 160;
      const v2u a0 = *(const v2u*)(ka + kg * 8), a1 = *(const v2u*)(ka + 32);
      const v2u b0 = *(const v2u*)(kb + kg * 8), b1_ = *(const v2u*)(kb + 32);
      const f32x4 sa = mfma(v4u{a0.x, a0.y, a1.x, a1.y}, qf, z);
      const f32x4 sb_ = mfma(v4u{b0.x, b0.y, b1_.x, b1_.y}, qf, z);
      const int rvb = 16 * u + 4 * kg + q4;
      const int rva = u > 0 ? rvb - 16 : rvb;
      const char* va = smem + rva * RS + 320 + h * 40 + pp * 8;
      const char* vb = smem + rvb * RS + 320 + h * 40 + pp * 8;
      const v2u va0 = trread(va), vb0 = trread(vb), va1 = trread(va + 32), vb1 = trread(vb + 32);
      const int kmin = (tI == 0 && u == 0) ? 7 : 0;
      float e[8];
      float mx = -3.0e38f;
#pragma unroll
      for (int r = 0; r < 4; ++r) {
        const int a = 4 * kg + r;
        const bool okA = (u > 0) && (a >= m16 + 9);
        const bool okB = (a <= m16) && (m16 <= a + 7) && (a >= kmin || a == m16);
        e[r] = okA ? sa[r] * sc : -3.0e38f;
        e[4 + r] = okB ? sb_[r] * sc : -3.0e38f;
        mx = fmaxf(mx, fmaxf(e[r], e[4 + r]));
      }
      mx = xmax16_32(mx);
      float sum = 0.f;
#pragma unroll
      for (int r = 0; r < 8; ++r) { e[r] = __builtin_amdgcn_exp2f(e[r] - mx); sum += e[r]; }
      sum = xsum16_32(sum);
      const float is = 1.0f / sum;
      const v4u pf = {pk2(e[0] * is, e[1] * is), pk2(e[2] * is, e[3] * is), pk2(e[4] * is, e[5] * is), pk2(e[6] * is, e[7] * is)};
      const f32x4 o0 = mfma(v4u{va0.x, va0.y, vb0.x, vb0.y}, pf, z);
      const f32x4 o1 = mfma(v4u{va1.x, va1.y, vb1.x, vb1.y}, pf, z);
      const int v = v0 + rq;
      if (rq >= 7 && rq < 121 && v <= vhi) {
        char* ob = ws + OFF_O + ((size_t)(v >> 6) * 80 + ((v >> 4) & 3)) * 1024 + (v & 15) * 16;
        const int c0 = chi + kg, c1 = chi + 4;
        *(v2u*)(ob + (c0 >> 3) * 4096 + (c0 & 3) * 256 + ((c0 >> 2) & 1) * 8) = v2u{pk2(o0[0], o0[1]), pk2(o0[2], o0[3])};
        if (kg == 0) *(v2u*)(ob + (c1 >> 3) * 4096 + (c1 & 3) * 256 + ((c1 >> 2) & 1) * 8) = v2u{pk2(o1[0], o1[1]), pk2(o1[2], o1[3])};
      }
    }
  }
}

__global__ __launch_bounds__(512, 2) void k1_kernel(const u16* __restrict__ x, const u16* __restrict__ wqkv,
                                                    const u16* __restrict__ w1, const u16* __restrict__ b1,
                                                    const u16* __restrict__ w2, const u16* __restrict__ wo,
                                                    char* __restrict__ ws) {
  extern __shared__ __attribute__((aligned(16))) char smem[];
  const int xcd = blockIdx.x & 7, idx = blockIdx.x >> 3;
  const int rh = xcd & 1, cq = xcd >> 1;
  STAMP(0, 0);
  if (idx < 18) {
    gemm_unit<true>(x, wqkv, b1, ws, smem, rh * 9 + (idx >> 1), cq * 2 + (idx & 1));
  } else if (idx < 31) {
    const int j = idx - 18;
    const int rt = j < 12 ? rh * 7 + (j >> 1) : 6;
    const int ct = cq * 2 + (j < 12 ? (j & 1) : rh);
    gemm_unit<false>(x, w1, b1, ws, smem, rt, ct);
  } else {
    const int tid = threadIdx.x, l = tid & 63;
    const int w = __builtin_amdgcn_readfirstlane(tid >> 6);
    const unsigned lds0 = (unsigned)(size_t)(LAS char*)smem;
    const int kg = l >> 4, q = (l >> 2) & 3, pp4 = l & 3;
    for (int r0 = 0; r0 < 17; r0 += 6) {
      const int nu = 17 - r0 < 6 ? 17 - r0 : 6;
      for (int pc = w; pc < nu * 20; pc += 8) {
        const int i = pc / 20, pp = pc % 20;
        const int u = xcd + 8 * (r0 + i), ks = u >> 1, hf = u & 1;
        const u16* src = ks < 48 ? w2 + (size_t)ks * 32 * D : wo + (size_t)(ks - 48) * 32 * D;
        const int e = pp * 64 + l, row = e / 40, ch = e % 40;
        glds16(src + row * D + hf * 320 + ch * 8, lds0 + i * 20480 + pp * 1024);
      }
      WAITV(0);
      __syncthreads();
      for (int fr = w; fr < nu * 20; fr += 8) {
        const int i = fr / 20, T = fr % 20;
        const int u = xcd + 8 * (r0 + i), ks = u >> 1, hf = u & 1;
        const char* base = smem + i * 20480 + (4 * kg + q) * 640 + T * 32 + pp4 * 8;
        const v2u a = trread(base), b = trread(base + 16 * 640);
        const int c = hf * 4 + T / 5, ct = T % 5;
        *(v4u*)(ws + OFF_WP + ((size_t)(c * 68 + ks) * 5 + ct) * 1024 + l * 16) = v4u{a.x, a.y, b.x, b.y};
      }
      __syncthreads();
    }
  }
  STAMP(0, 3);
}

constexpr int K2_LDS = 4 * 20 * 1024;
constexpr int GD = 6;

template <int NK>
DEVI void stream(const char* ap, const char* bp, int rot, f32x4 (&acc)[4][5]) {
  v4u ra[GD][4], rb[GD][5];
  auto ld = [&](int d, int k) {
    int kk = k + rot;
    kk = kk >= NK ? kk - NK : kk;
    const char* a = ap + kk * 4096;
    const char* b = bp + kk * 5120;
#pragma unroll
    for (int j = 0; j < 5; ++j) rb[d][j] = *(const v4u*)(b + j * 1024);
#pragma unroll
    for (int j = 0; j < 4; ++j) ra[d][j] = *(const v4u*)(a + j * 1024);
  };
#pragma unroll
  for (int d = 0; d < GD; ++d) ld(d, d);
  SB();
#pragma unroll
  for (int k = 0; k < NK; ++k) {
    const int d = k % GD;
#pragma unroll
    for (int a = 0; a < 4; ++a)
#pragma unroll
      for (int b = 0; b < 5; ++b) acc[a][b] = mfma(rb[d][b], ra[d][a], acc[a][b]);
    if (k + GD < NK) ld(d, k + GD);
    SB();
  }
}

__global__ __launch_bounds__(256, 1) void k2_kernel(const u16* __restrict__ x, const u16* __restrict__ b2,
                                                    const char* __restrict__ ws, u16* __restrict__ out) {
  extern __shared__ __attribute__((aligned(16))) char smem[];
  const int tid = threadIdx.x, l = tid & 63;
  const int w = __builtin_amdgcn_readfirstlane(tid >> 6);
  const int xcd = blockIdx.x & 7, idx = blockIdx.x >> 3;
#define K2MAP 0
  const int pl = idx >> 3, c = idx & 7, p = xcd * 4 + pl;
  const int rot = (2 * c + 4 * pl) & 15;
  const int m16 = l & 15, kg = l >> 4;
  STAMP(1, 0);
  const char* wp = ws + OFF_WP + (size_t)c * 68 * 5120 + l * 16;
  f32x4 acc[4][5];
#pragma unroll
  for (int a = 0; a < 4; ++a)
#pragma unroll
    for (int b = 0; b < 5; ++b) acc[a][b] = f32x4{0.f, 0.f, 0.f, 0.f};
  const int row = vrow2row(64 * p + 16 * w + m16);
  v2u xr[5], br[5];
#pragma unroll
  for (int b = 0; b < 5; ++b) {
    xr[b] = *(const v2u*)(x + (size_t)row * D + 80 * c + 16 * b + 4 * kg);
    br[b] = *(const v2u*)(b2 + 80 * c + 16 * b + 4 * kg);
  }
  const int rt_ = 0;
  if (w < 3) stream<16>(ws + OFF_H2 + ((size_t)p * 48 + 16 * w) * 4096 + l * 16, wp + (size_t)16 * w * 5120, rt_, acc);
  else stream<20>(ws + OFF_O + (size_t)p * 20 * 4096 + l * 16, wp + (size_t)48 * 5120, rt_, acc);
  STAMP(1, 2);
  f32x4* red = (f32x4*)smem;
#pragma unroll
  for (int a = 0; a < 4; ++a)
#pragma unroll
    for (int b = 0; b < 5; ++b) red[((w * 4 + a) * 5 + b) * 64 + l] = acc[a][b];
  __syncthreads();
  u16* op = out + (size_t)row * D + 80 * c + 4 * kg;
#pragma unroll
  for (int b = 0; b < 5; ++b) {
    const f32x4 g0 = red[((0 * 4 + w) * 5 + b) * 64 + l], g1 = red[((1 * 4 + w) * 5 + b) * 64 + l];
    const f32x4 g2 = red[((2 * 4 + w) * 5 + b) * 64 + l], sv = red[((3 * 4 + w) * 5 + b) * 64 + l];
    float r[4];
    const float bb[4] = {bflo(br[b].x), bfhi(br[b].x), bflo(br[b].y), bfhi(br[b].y)};
    const float xx[4] = {bflo(xr[b].x), bfhi(xr[b].x), bflo(xr[b].y), bfhi(xr[b].y)};
#pragma unroll
    for (int j = 0; j < 4; ++j) {
      const float g = (g0[j] + g1[j]) + g2[j] + bb[j];
      const float si = g / (1.0f + __expf(-g));
      r[j] = xx[j] + sv[j] * si;
    }
    *(v2u*)(op + 16 * b) = v2u{pk2(r[0], r[1]), pk2(r[2], r[3])};
  }
  STAMP(1, 3);
}

extern "C" void kernel_launch(void* const* d_in, const int* in_sizes, int n_in, void* d_out, int out_size,
                              void* d_ws, size_t ws_size, hipStream_t stream) {
  const u16* x = (const u16*)d_in[0];
  const u16* wqkv = (const u16*)d_in[1];
  const u16* wo = (const u16*)d_in[2];
  const u16* w1 = (const u16*)d_in[4];
  const u16* b1 = (const u16*)d_in[5];
  const u16* w2 = (const u16*)d_in[6];
  const u16* b2 = (const u16*)d_in[7];
  (void)hipFuncSetAttribute((const void*)k1_kernel, hipFuncAttributeMaxDynamicSharedMemorySize, K1_LDS);
  (void)hipFuncSetAttribute((const void*)k2_kernel, hipFuncAttributeMaxDynamicSharedMemorySize, 90112);
  k1_kernel<<<256, 512, K1_LDS, stream>>>(x, wqkv, w1, b1, w2, wo, (char*)d_ws);
  k2_kernel<<<256, 256, 90112, stream>>>(x, b2, (const char*)d_ws, (u16*)d_out);
}
```

```cpp
#include <hip/hip_runtime.h>
#include <hip/hip_bf16.h>

typedef unsigned short u16;
typedef short bf16x8 __attribute__((ext_vector_type(8)));
typedef short bf16x4 __attribute__((ext_vector_type(4)));
typedef float f32x4 __attribute__((ext_vector_type(4)));
typedef unsigned v4u __attribute__((ext_vector_type(4)));
typedef unsigned v2u __attribute__((ext_vector_type(2)));
typedef __bf16 bfx2 __attribute__((ext_vector_type(2)));
typedef float f32x2 __attribute__((ext_vector_type(2)));
typedef __amdgpu_buffer_rsrc_t rsrc_t;
#define LAS __attribute__((address_space(3)))
#define GAS __attribute__((address_space(1)))
#define DEVI __device__ __forceinline__

constexpr int S = 2048, D = 640, F = 1536;
constexpr int OFF_R = 0;
constexpr int OFF_FLAG = 4096;
constexpr int OFF_STAT = OFF_FLAG + 262144;
constexpr int OFF_H2 = OFF_STAT + 327680;
constexpr int OFF_O = OFF_H2 + 6291456;
constexpr int OFF_WP = OFF_O + 2621440;
constexpr int WS_USED = OFF_WP + 2785280;
constexpr int SC1 = 16;
constexpr unsigned SPIN_CAP = 1u << 18;

#define O8 0x3F80, 0x3F80, 0x3F80, 0x3F80, 0x3F80, 0x3F80, 0x3F80, 0x3F80
#define O64 O8, O8, O8, O8, O8, O8, O8, O8
static __device__ const __attribute__((aligned(128))) u16 ONES_ROW[640] = {O64, O64, O64, O64, O64, O64, O64, O64, O64, O64};

DEVI float bfhi(unsigned u) { return __uint_as_float(u & 0xFFFF0000u); }
DEVI float bflo(unsigned u) { return __uint_as_float(u << 16); }
DEVI unsigned pk2(float a, float b) {
  f32x2 v = {a, b};
  bfx2 r = __builtin_convertvector(v, bfx2);
  return __builtin_bit_cast(unsigned, r);
}
DEVI float rbf(float a) { return bflo(pk2(a, a)); }
DEVI f32x4 mfma(v4u a, v4u b, f32x4 c) {
  return __builtin_amdgcn_mfma_f32_16x16x32_bf16(__builtin_bit_cast(bf16x8, a), __builtin_bit_cast(bf16x8, b), c, 0, 0, 0);
}
DEVI v2u trread(const char* p) {
  bf16x4 r = __builtin_amdgcn_ds_read_tr16_b64_v4i16((LAS bf16x4*)p);
  return __builtin_bit_cast(v2u, r);
}
DEVI float xsum16_32(float v) {
  auto a = __builtin_amdgcn_permlane16_swap(__float_as_uint(v), __float_as_uint(v), false, false);
  v = __uint_as_float(a[0]) + __uint_as_float(a[1]);
  auto b = __builtin_amdgcn_permlane32_swap(__float_as_uint(v), __float_as_uint(v), false, false);
  return __uint_as_float(b[0]) + __uint_as_float(b[1]);
}
DEVI float xmax16_32(float v) {
  auto a = __builtin_amdgcn_permlane16_swap(__float_as_uint(v), __float_as_uint(v), false, false);
  v = fmaxf(__uint_as_float(a[0]), __uint_as_float(a[1]));
  auto b = __builtin_amdgcn_permlane32_swap(__float_as_uint(v), __float_as_uint(v), false, false);
  return fmaxf(__uint_as_float(b[0]), __uint_as_float(b[1]));
}
template <int CTRL> DEVI float dpp(float x) {
  return __int_as_float(__builtin_amdgcn_mov_dpp(__float_as_int(x), CTRL, 0xf, 0xf, true));
}
DEVI float wsum64(float v) {
  v += dpp<0xB1>(v);
  v += dpp<0x4E>(v);
  v += dpp<0x141>(v);
  v += dpp<0x128>(v);
  return xsum16_32(v);
}
DEVI unsigned next_tag(unsigned r) { return r * 1664525u + 1013904223u; }
#define WAITV(n) asm volatile("s_waitcnt vmcnt(%0)" ::"i"(n) : "memory")
#define WAITL0() __builtin_amdgcn_s_waitcnt(0xC07F)
#define SB() __builtin_amdgcn_sched_barrier(0)
#define BAR() __builtin_amdgcn_s_barrier()
DEVI void glds16(const void* g, unsigned lds_dst) {
  unsigned keep;
  asm volatile("s_mov_b32 %0, m0\n\ts_mov_b32 m0, %2\n\ts_nop 0\n\tglobal_load_lds_dwordx4 %1, off\n\ts_mov_b32 m0, %0"
               : "=&s"(keep) : "v"(g), "s"(lds_dst) : "memory");
}
DEVI int vrow2row(int v) { return ((v & 1023) << 1) | (v >> 10); }
#define RLX_AGENT __ATOMIC_RELAXED, __HIP_MEMORY_SCOPE_AGENT
DEVI unsigned ld32(const char* ws, int off) { return __hip_atomic_load((const GAS unsigned*)(ws + off), RLX_AGENT); }
DEVI v2u ld64(const char* ws, int off) {
  const unsigned long long v = __hip_atomic_load((const GAS unsigned long long*)(ws + off), RLX_AGENT);
  return v2u{(unsigned)v, (unsigned)(v >> 32)};
}
DEVI v4u ld128(rsrc_t rs, int off, int soff) { return __builtin_amdgcn_raw_buffer_load_b128(rs, off, soff, SC1); }
DEVI void st32(rsrc_t rs, int off, unsigned v) { __builtin_amdgcn_raw_buffer_store_b32(v, rs, off, 0, SC1); }
DEVI void st64(rsrc_t rs, int off, v2u v) { __builtin_amdgcn_raw_buffer_store_b64(v, rs, off, 0, SC1); }
DEVI void st128(rsrc_t rs, int off, v4u v) { __builtin_amdgcn_raw_buffer_store_b128(v, rs, off, 0, SC1); }
constexpr int OOB = 0x7FFFFFF0;

DEVI int blk_qkv(int par, int tI, int c) { return ((tI * 2 + (c & 1)) << 3) | ((c >> 1) * 2 + par); }
DEVI int blk_h(int rt, int ct) {
  const int e = ct & 1, rh = rt < 6 ? 0 : (rt > 6 ? 1 : e);
  const int j = rt == 6 ? 12 : (rt - 7 * rh) * 2 + e;
  return ((18 + j) << 3) | ((ct >> 1) * 2 + rh);
}
DEVI int blk_p2(int p, int c) { return (((p & 3) * 8 + c) << 3) | (p >> 2); }

#define STAMP(k, i)
#define STAMPW(k, i, t)

#define ROT1(x) (x)
constexpr int STAGE = 47104;
constexpr int L_RP = 3 * STAGE;
constexpr int L_CS = L_RP + 10240;
constexpr int LDS_BYTES = 163840;
constexpr int RS = 528;
constexpr int PW = 4, PA = 3;

struct Ctx {
  const u16* x; const u16* w2; const u16* wo; const char* ws;
  char* smem; unsigned lds0; rsrc_t rs; int b, w, l;
};

DEVI void repack_issue(const Ctx& c, int u, unsigned ldsoff) {
  const int ks = u >> 2, qd = u & 3;
  const u16* src = (ks < 48 ? c.w2 + ks * 32 * D : c.wo + (ks - 48) * 32 * D) + qd * 160;
  for (int pc = c.w; pc < 10; pc += 8) {
    const int e = pc * 64 + c.l, row = e / 20, ch = e % 20;
    glds16(src + row * D + ch * 8, c.lds0 + ldsoff + pc * 1024);
  }
}
DEVI void repack_store(const Ctx& c, int u, unsigned ldsoff) {
  const int ks = u >> 2, qd = u & 3;
  const int kg = c.l >> 4, q = (c.l >> 2) & 3, pp = c.l & 3;
  for (int T = c.w; T < 10; T += 8) {
    const char* base = c.smem + ldsoff + (4 * kg + q) * 320 + T * 32 + pp * 8;
    const v2u a = trread(base), b = trread(base + 16 * 320);
    const int cg = qd * 2 + (T >= 5 ? 1 : 0), ct = T >= 5 ? T - 5 : T;
    st128(c.rs, OFF_WP + ((cg * 68 + ks) * 5 + ct) * 1024 + c.l * 16, v4u{a.x, a.y, b.x, b.y});
  }
}
DEVI int repack_cons(const Ctx& c, int u) { return blk_p2(c.l & 31, (u & 3) * 2 + (c.l >> 5)); }
DEVI int repack_flagoff(int u, int cb) {
  const int ks = u >> 2;
  const int cw = ks < 48 ? ks >> 3 : (ks < 58 ? 6 : 7);
  const int sl = ks < 48 ? ks & 7 : (ks < 58 ? ks - 48 : ks - 58);
  return OFF_FLAG + (cb * 8 + cw) * 128 + sl * 4;
}

struct StatLd { v4u a, b; unsigned r, r2, my; int dst; };
DEVI void stat_issue(const Ctx& c, StatLd& s, int cb2) {
  const int v = c.b * 8 + c.w, l = c.l;
  const u16* rp = c.x + vrow2row(v) * D;
  const int par = v >> 10, J = v & 1023, tI = J / 114, rem = J - 114 * tI;
  const int hrt = v / 158;
  int cb, slot;
  bool ok = l < 24;
  if (l < 8) { cb = blk_qkv(par, tI, l); slot = rem + 7; }
  else if (l < 16) { cb = blk_qkv(par, tI + 1, l - 8); slot = rem - 107; ok = rem >= 107 && tI < 8; }
  else { cb = blk_h(hrt, l & 7); slot = v - 158 * hrt; }
  cb = ok ? cb : 0;
  s.dst = ok ? OFF_STAT + (cb * 160 + slot) * 8 : OOB;
  const void* p0 = rp + l * 8;
  const void* p1 = rp + 512 + (l & 15) * 8;
  const void* p2 = c.ws + OFF_R + cb * 4;
  const void* p3 = c.ws + OFF_R + cb2 * 4;
  const void* p4 = c.ws + OFF_R + c.b * 4 + (l & 0);
  asm volatile("global_load_dwordx4 %0, %5, off\n\tglobal_load_dwordx4 %1, %6, off\n\tglobal_load_dword %2, %7, off sc1\n\t"
               "global_load_dword %3, %8, off sc1\n\tglobal_load_dword %4, %9, off sc1"
               : "=&v"(s.a), "=&v"(s.b), "=&v"(s.r), "=&v"(s.r2), "=&v"(s.my)
               : "v"(p0), "v"(p1), "v"(p2), "v"(p3), "v"(p4) : "memory");
}
DEVI void stat_finish(const Ctx& c, StatLd& s) {
  asm volatile("" : "+v"(s.a), "+v"(s.b), "+v"(s.r), "+v"(s.r2), "+v"(s.my));
  float e[16];
  const float m2 = c.l < 16 ? 1.f : 0.f;
#pragma unroll
  for (int j = 0; j < 4; ++j) { e[2 * j] = bflo(s.a[j]); e[2 * j + 1] = bfhi(s.a[j]); e[8 + 2 * j] = bflo(s.b[j]); e[9 + 2 * j] = bfhi(s.b[j]); }
  float s0 = 0.f, s1 = 0.f;
#pragma unroll
  for (int j = 0; j < 8; ++j) { s0 += e[j]; s1 += e[8 + j]; }
  const float mean = wsum64(s0 + m2 * s1) * (1.0f / D);
  s0 = 0.f; s1 = 0.f;
#pragma unroll
  for (int j = 0; j < 8; ++j) { const float d0 = e[j] - mean, d1 = e[8 + j] - mean; s0 = fmaf(d0, d0, s0); s1 = fmaf(d1, d1, s1); }
  const float var = wsum64(s0 + m2 * s1) * (1.0f / D);
  const float den = sqrtf(rbf(rbf(var) + 1e-5f));
  st64(c.rs, s.dst, v2u{pk2(mean, den), next_tag(s.r)});
}

template <bool QKV, typename PreEpi>
DEVI void gemm_unit(const Ctx& c, const u16* __restrict__ W, const u16* __restrict__ b1, int rt, int ct, int rot,
                    unsigned& mytag, PreEpi pre_epi) {
  constexpr int MI = QKV ? 2 : 5, NI = QKV ? 8 : 3;
  constexpr int NA = QKV ? 16 : 20;
  constexpr int NP = QKV ? 46 : 44;
  constexpr int BOFF = NA * 1024;
  constexpr int LDW = QKV ? 3 * D : F;
  constexpr int ROWS = NA * 8;
  constexpr int NM = QKV ? 14 : 15;
  constexpr int NR = MI + 2 * NI;
  __builtin_amdgcn_s_waitcnt(0x0F70);
  const int l = c.l, w = c.w;
  char* smem = c.smem;
  const int wm = QKV ? (w & 3) : (w >> 2), wn = QKV ? (w >> 2) : (w & 3);
  const int m16 = l & 15, kg = l >> 4;
  const int par = QKV ? (rt >= 9 ? 1 : 0) : 0;
  const int tI = QKV ? rt - 9 * par : 0;
  const int v0 = QKV ? par * 1024 + 114 * tI - 7 : 158 * rt;
  const int vlo = QKV ? par * 1024 : 0, vhi = QKV ? par * 1024 + 1023 : 2047;

  const char* gp[6];
#pragma unroll
  for (int i = 0; i < 6; ++i) {
    const int pc = w + 8 * i;
    if (i < 2 || (!QKV && i == 2 && w < 4)) {
      const int j = l >> 3, pos = l & 7;
      const int lr = pc * 8 + j;
      int v = v0 + lr;
      v = v < vlo ? vlo : (v > vhi ? vhi : v);
      const u16* rp = lr == ROWS - 1 ? ONES_ROW : c.x + vrow2row(v) * D;
      gp[i] = (const char*)rp + ((pos ^ ((lr >> 1) & 7)) << 4);
    } else if (QKV) {
      const int pb = pc - NA < 30 ? pc - NA : 29;
      const int e = pb * 64 + l, r = e / 30, s16 = e % 30;
      int T = (s16 >> 1) + 4 * ((r >> 3) & 1);
      T = T >= 15 ? T - 15 : T;
      gp[i] = (const char*)(W + r * LDW + (T / 5) * D + ct * 80 + (T % 5) * 16 + (s16 & 1) * 8);
    } else {
      const int j = l >> 3, pos = l & 7;
      const int pb = pc - NA < 24 ? pc - NA : 23;
      const int kb = pb / 3, Pp = pb % 3;
      const int c32 = (pos >> 1) ^ (((j >> 1) & 1) | ((kb & 1) << 1));
      gp[i] = (const char*)(W + (kb * 8 + j) * LDW + ct * 192 + Pp * 64) + c32 * 32 + (pos & 1) * 16;
    }
  }
  const bool a2 = !QKV && w < 4;
  const bool has6 = w + 40 < NP;
  auto issue1 = [&](int i, unsigned lb, int kst) {
    if (i < 5 || has6) {
      const int off = (i < 2 || (i == 2 && a2)) ? kst * 128 : kst * (LDW * 128);
      glds16(gp[i] + off, lb + i * 8192);
    }
  };
  const int ru = (c.b >> 3) * 8 + (c.b & 7);
  int kst = rot;
  StatLd sl;
  const int rcb = repack_cons(c, ru);
  stat_issue(c, sl, rcb);
#pragma unroll
  for (int i = 0; i < 6; ++i) issue1(i, c.lds0 + w * 1024, kst);
  kst = kst == 9 ? 0 : kst + 1;
  STAMP(0, 1);
  repack_issue(c, ru, L_RP);
#pragma unroll
  for (int i = 0; i < 6; ++i) issue1(i, c.lds0 + STAGE + w * 1024, kst);
  kst = kst == 9 ? 0 : kst + 1;

  const int fsw = m16 >> 1;
  const int xrow = (wm * (MI * 16) + m16) * 128;
  const int xa0 = xrow + ((kg ^ fsw) << 4), xa1 = xrow + (((4 + kg) ^ fsw) << 4);
  int wb[3];
  {
    const int q = (l >> 2) & 3, pp = l & 3;
    if (QKV) {
      const int base = BOFF + (8 * kg + q) * 480 + pp * 8 + wn * 256;
      wb[1] = base - 128 * (kg & 1);
      wb[0] = wn == 0 ? base + 352 * (kg & 1) : wb[1];
      wb[2] = 0;
    } else {
      const int swz = ((q >> 1) & 1) | ((kg & 1) << 1);
#pragma unroll
      for (int ni = 0; ni < 3; ++ni) {
        const int T = wn * 3 + ni;
        wb[ni] = BOFF + (kg * 3 + (T >> 2)) * 1024 + q * 128 + (((T & 3) ^ swz) << 5) + pp * 8;
      }
    }
  }

  f32x4 acc[MI][NI];
#pragma unroll
  for (int a = 0; a < MI; ++a)
#pragma unroll
    for (int b = 0; b < NI; ++b) acc[a][b] = f32x4{0.f, 0.f, 0.f, 0.f};
  v4u xf[2][MI];
  v2u wf[2][NI][2];
#pragma unroll
  for (int a = 0; a < MI; ++a) xf[1][a] = v4u{0u, 0u, 0u, 0u};
#pragma unroll
  for (int b = 0; b < NI; ++b) { wf[1][b][0] = v2u{0u, 0u}; wf[1][b][1] = v2u{0u, 0u}; }

  WAITV(10);
  stat_finish(c, sl);
  mytag = next_tag(sl.my);
  STAMP(0, 2);

  auto rd1 = [&](int set, int r, const char* sb, int kk) {
    if (r < MI) xf[set][r] = *(const v4u*)(sb + (kk ? xa1 : xa0) + r * 2048);
    else if (r < NR) {
      const int ni = (r - MI) >> 1, t = (r - MI) & 1;
      const char* p = QKV ? sb + wb[ni < 4 ? 0 : 1] + ni * 32 + kk * 15360 + t * 1920 : sb + wb[ni] + kk * 12288 + t * 512;
      wf[set][ni][t] = trread(p);
    }
  };
  auto mm1 = [&](int set, int j) {
    const int ni = j / MI, mi = j % MI;
    acc[mi][ni] = mfma(v4u{wf[set][ni][0].x, wf[set][ni][0].y, wf[set][ni][1].x, wf[set][ni][1].y}, xf[set][mi], acc[mi][ni]);
  };
  auto half = [&](int set, const char* sb, int kk, bool rd, bool more, unsigned lb, int i0) {
#pragma unroll
    for (int j = 0; j < NM; ++j) {
      mm1(set, j);
      if (rd) { rd1(set ^ 1, 2 * j, sb, kk); rd1(set ^ 1, 2 * j + 1, sb, kk); }
      SB();
      if (j == 3 || j == 8 || j == 12) {
        if (more) issue1(i0 + (j == 3 ? 0 : j == 8 ? 1 : 2), lb, kst);
        SB();
      }
    }
    if (QKV && wn == 0) { mm1(set, 14); mm1(set, 15); }
    SB();
  };

  int buf = 0;
  int nloop = 9;
  asm volatile("" : "+s"(nloop));
  for (int s = 0; s < nloop; ++s) {
    if (has6) WAITV(6); else WAITV(5);
    WAITL0();
    BAR();
    if (s == 0) { STAMP(0, 3); repack_store(c, ru, L_RP); }
    if (s == 1 && w == 0) st32(c.rs, repack_flagoff(ru, rcb), next_tag(sl.r2));
    const char* sb = smem + buf * STAGE;
    const unsigned lb = c.lds0 + (buf == 0 ? 2 : buf - 1) * STAGE + w * 1024;
    const bool more = s < 8;
    SB();
    half(1, sb, 0, true, more, lb, 0);
    half(0, sb, 1, true, more, lb, 3);
    buf = buf == 2 ? 0 : buf + 1;
    kst = kst == 9 ? 0 : kst + 1;
  }
  WAITV(0);
  WAITL0();
  BAR();
  v2u sg[MI];
  int soff[MI];
#pragma unroll
  for (int mi = 0; mi < MI; ++mi) {
    const int lr = wm * (MI * 16) + mi * 16 + m16, v = v0 + lr;
    const bool need = lr < (QKV ? 121 : 158) && v >= vlo && v <= vhi;
    soff[mi] = need ? 1 : 0;
    sg[mi] = ld64(c.ws, OFF_STAT + (c.b * 160 + lr) * 8);
  }
  v2u bias[3];
  if (!QKV) {
#pragma unroll
    for (int ni = 0; ni < 3; ++ni) bias[ni] = *(const v2u*)(b1 + ct * 192 + wn * 48 + ni * 16 + kg * 4);
  }
  int fdst;
  {
    const int vfirst = QKV ? v0 + 7 : v0, vlast = (vfirst + (QKV ? 113 : 157)) > vhi ? vhi : vfirst + (QKV ? 113 : 157);
    const int p = (vfirst >> 6) + ((l >> 3) & 3), cc = l & 7;
    const int cb = blk_p2(p & 31, cc);
    const int second = p * 64 >= vfirst ? 0 : 1;
    int cw, sl2;
    bool ok = p <= (vlast >> 6);
    if (QKV) { cw = 6 + (ct >> 2); sl2 = 16 + (ct & 3) * 2 + second; ok = ok && l < 32; }
    else {
      const int cw0 = (192 * ct) >> 8, cw1 = (192 * ct + 191) >> 8;
      cw = l < 32 ? cw0 : cw1;
      ok = ok && (l < 32 || cw1 != cw0);
      sl2 = 16 + (ct - (cw * 4) / 3) * 2 + second;
    }
    fdst = ok ? OFF_FLAG + (cb * 8 + cw) * 128 + sl2 * 4 : OOB;
    sl.r = ld32(c.ws, OFF_R + cb * 4);
  }
  const unsigned rpf = pre_epi(0, 0u);
  SB();
  {
    const char* sb = smem + buf * STAGE;
    half(1, sb, 0, true, false, 0u, 0);
    half(0, sb, 1, true, false, 0u, 3);
    half(1, sb, 0, false, false, 0u, 0);
  }
  STAMP(0, 4);
  float* csl = (float*)(smem + L_CS);
  if (wm == (QKV ? 3 : 1) && m16 == 15) {
#pragma unroll
    for (int ni = 0; ni < NI; ++ni) *(f32x4*)(csl + (wn * NI + ni) * 16 + kg * 4) = acc[MI - 1][ni];
  }
  float c0[MI], inv[MI];
  auto stat_ok = [&]() {
    bool ok = true;
#pragma unroll
    for (int mi = 0; mi < MI; ++mi) ok = ok && (sg[mi].y == mytag || !soff[mi]);
    return __all(ok) != 0;
  };
  if (!stat_ok()) {
    _Pragma("unroll 1") for (unsigned spins = 0; spins < SPIN_CAP; ++spins) {
#pragma unroll
      for (int mi = 0; mi < MI; ++mi) sg[mi] = ld64(c.ws, OFF_STAT + (c.b * 160 + wm * (MI * 16) + mi * 16 + m16) * 8);
      if (stat_ok()) break;
    }
  }
  pre_epi(1, rpf);
  STAMP(0, 5);
#pragma unroll
  for (int mi = 0; mi < MI; ++mi) {
    inv[mi] = soff[mi] ? __builtin_amdgcn_rcpf(bfhi(sg[mi].x)) : 0.f;
    c0[mi] = soff[mi] ? -bflo(sg[mi].x) * inv[mi] : 0.f;
  }
  WAITL0();
  BAR();
  if (!QKV) {
    f32x4 cb[NI];
#pragma unroll
    for (int ni = 0; ni < NI; ++ni) cb[ni] = *(const f32x4*)(csl + (wn * NI + ni) * 16 + kg * 4);
    const int t0 = ct * 6 + ((3 * wn) >> 1);
#pragma unroll
    for (int mi = 0; mi < MI; ++mi) {
      const int v = v0 + wm * 80 + mi * 16 + m16;
      v2u pk[3];
#pragma unroll
      for (int ni = 0; ni < 3; ++ni) {
        const float bb[4] = {bflo(bias[ni].x), bfhi(bias[ni].x), bflo(bias[ni].y), bfhi(bias[ni].y)};
        float t[4];
#pragma unroll
        for (int r = 0; r < 4; ++r) {
          const float u = fmaxf(fmaf(acc[mi][ni][r], inv[mi], fmaf(c0[mi], cb[ni][r], bb[r])), 0.f);
          t[r] = u * u;
        }
        pk[ni] = v2u{pk2(t[0], t[1]), pk2(t[2], t[3])};
      }
      const int hb = !soff[mi] ? OOB - 16384 : OFF_H2 + (((v >> 6) * 48 + t0) * 4 + ((v >> 4) & 3)) * 1024 + (kg * 16 + (v & 15)) * 16;
      if (wn & 1) {
        st64(c.rs, hb + 8, pk[0]);
        st128(c.rs, hb + 4096, v4u{pk[1].x, pk[1].y, pk[2].x, pk[2].y});
      } else {
        st128(c.rs, hb, v4u{pk[0].x, pk[0].y, pk[1].x, pk[1].y});
        st64(c.rs, hb + 4096, pk[2]);
      }
    }
  } else {
#pragma unroll
    for (int ni = 0; ni < NI; ++ni) {
      const f32x4 cs = *(const f32x4*)(csl + (wn * NI + ni) * 16 + kg * 4);
#pragma unroll
      for (int mi = 0; mi < MI; ++mi) {
        float t[4];
#pragma unroll
        for (int r = 0; r < 4; ++r) t[r] = fmaf(acc[mi][ni][r], inv[mi], c0[mi] * cs[r]);
        *(v2u*)(smem + (wm * 32 + mi * 16 + m16) * RS + (wn * 8 + ni) * 32 + kg * 8) = v2u{pk2(t[0], t[1]), pk2(t[2], t[3])};
      }
    }
    WAITL0();
    BAR();
    const int h = w & 3;
    const int q4 = (l >> 2) & 3, pp = l & 3;
    const float sc = 0.22360679774997896f * 1.4426950408889634f;
    const f32x4 z = {0.f, 0.f, 0.f, 0.f};
    const int ch0 = 20 * ct + 5 * h + kg, ch1 = 20 * ct + 5 * h + 4;
    const int oo0 = (ch0 >> 3) * 4096 + (ch0 & 3) * 256 + ((ch0 >> 2) & 1) * 8;
    const int oo1 = (ch1 >> 3) * 4096 + (ch1 & 3) * 256 + ((ch1 >> 2) & 1) * 8;
#pragma unroll 1
    for (int u = (w >> 2) * 4; u < (w >> 2) * 4 + 4; ++u) {
      const int rq = 16 * u + m16;
      const int back = u > 0 ? 16 * RS : 0;
      const char* qp = smem + rq * RS + h * 40;
      v2u q0 = *(const v2u*)(qp + kg * 8), q1 = *(const v2u*)(qp + 32);
      if (kg != 0) q1 = v2u{0u, 0u};
      const v4u qf = {q0.x, q0.y, q1.x, q1.y};
      const char* kb = qp + 160;
      const char* ka = kb - back;
      const v2u a0 = *(const v2u*)(ka + kg * 8), a1 = *(const v2u*)(ka + 32);
      const v2u b0 = *(const v2u*)(kb + kg * 8), b1_ = *(const v2u*)(kb + 32);
      const char* vb = smem + (16 * u + 4 * kg + q4) * RS + 320 + h * 40 + pp * 8;
      const char* va = vb - back;
      const v2u va0 = trread(va), vb0 = trread(vb), va1 = trread(va + 32), vb1 = trread(vb + 32);
      const f32x4 sa = mfma(v4u{a0.x, a0.y, a1.x, a1.y}, qf, z);
      const f32x4 sb_ = mfma(v4u{b0.x, b0.y, b1_.x, b1_.y}, qf, z);
      const int kmin = (tI == 0 && u == 0) ? 7 : 0;
      float e[8];
      float mx = -3.0e38f;
#pragma unroll
      for (int r = 0; r < 4; ++r) {
        const int a = 4 * kg + r;
        const bool okA = (u > 0) && (a >= m16 + 9);
        const bool okB = (a <= m16) && (m16 <= a + 7) && (a >= kmin || a == m16);
        e[r] = okA ? sa[r] * sc : -3.0e38f;
        e[4 + r] = okB ? sb_[r] * sc : -3.0e38f;
        mx = fmaxf(mx, fmaxf(e[r], e[4 + r]));
      }
      mx = xmax16_32(mx);
      float sum = 0.f;
#pragma unroll
      for (int r = 0; r < 8; ++r) { e[r] = __builtin_amdgcn_exp2f(e[r] - mx); sum += e[r]; }
      sum = xsum16_32(sum);
      const float is = __builtin_amdgcn_rcpf(sum);
      const v4u pf = {pk2(e[0] * is, e[1] * is), pk2(e[2] * is, e[3] * is), pk2(e[4] * is, e[5] * is), pk2(e[6] * is, e[7] * is)};
      const f32x4 o0 = mfma(v4u{va0.x, va0.y, vb0.x, vb0.y}, pf, z);
      const f32x4 o1 = mfma(v4u{va1.x, va1.y, vb1.x, vb1.y}, pf, z);
      const int v = v0 + rq;
      const bool ok = rq >= 7 && rq < 121 && v <= vhi;
      const int ob = OFF_O + ((v >> 6) * 80 + ((v >> 4) & 3)) * 1024 + (v & 15) * 16;
      st64(c.rs, ok ? ob + oo0 : OOB, v2u{pk2(o0[0], o0[1]), pk2(o0[2], o0[3])});
      st64(c.rs, ok && kg == 0 ? ob + oo1 : OOB, v2u{pk2(o1[0], o1[1]), pk2(o1[2], o1[3])});
    }
  }
  STAMP(0, 6);
  WAITV(0);
  BAR();
  if (w == 0) st32(c.rs, fdst, next_tag(sl.r));
  STAMP(0, 7);
}

__global__ __launch_bounds__(512, 2) void fused_kernel(const u16* __restrict__ x, const u16* __restrict__ wqkv,
                                                       const u16* __restrict__ wo, const u16* __restrict__ w1,
                                                       const u16* __restrict__ b1, const u16* __restrict__ w2,
                                                       const u16* __restrict__ b2, char* __restrict__ ws,
                                                       u16* __restrict__ out) {
  extern __shared__ __attribute__((aligned(16))) char smem[];
  Ctx c;
  c.x = x; c.w2 = w2; c.wo = wo; c.smem = smem; c.ws = ws;
  c.lds0 = (unsigned)(size_t)(LAS char*)smem;
  c.rs = __builtin_amdgcn_make_buffer_rsrc(ws, 0, WS_USED, 0x00020000);
  c.b = blockIdx.x;
  c.l = threadIdx.x & 63;
  c.w = __builtin_amdgcn_readfirstlane(threadIdx.x >> 6);
  const int l = c.l, w = c.w;
  const int xcd = c.b & 7, idx = c.b >> 3;
  const int rh = xcd & 1, cq = xcd >> 1;
  STAMP(0, 0);
  unsigned mytag;

  const int pl = idx >> 3, cg = idx & 7, p = xcd * 4 + pl;
  const int nk = w < 6 ? 8 : 10;
  const int ks0 = w < 6 ? 8 * w : 48 + 10 * (w - 6);
  const int rot2 = (cg + 2 * pl) & 7;
  const int woff = OFF_WP + (cg * 68 + ks0) * 5120;
  const int aoff = w < 6 ? OFF_H2 + (p * 48 + ks0) * 4096 : OFF_O + (p * 20 + ks0 - 48) * 4096;
  const int foff = OFF_FLAG + (c.b * 8 + w) * 128;
  v4u rb[PW][5];
  auto kidx = [&](int k) { int kk = k + rot2; return kk >= nk ? kk - nk : kk; };
  auto ldw = [&](int d, int k) {
    const int so = woff + kidx(k) * 5120;
#pragma unroll
    for (int j = 0; j < 5; ++j) rb[d][j] = ld128(c.rs, l * 16 + j * 1024, so);
  };
  auto pre_epi = [&](int step, unsigned v) -> unsigned {
    const int fo = foff + (l & 15) * 4;
    if (step == 0) return ld32(c.ws, fo);
    if (!__all(v == mytag || l >= nk)) {
      _Pragma("unroll 1") for (unsigned spins = 0; spins < SPIN_CAP; ++spins) {
        v = ld32(c.ws, fo);
        if (__all(v == mytag || l >= nk)) break;
      }
    }
    __builtin_amdgcn_fence(__ATOMIC_ACQUIRE, "wavefront");
#pragma unroll
    for (int d = 0; d < PW; ++d) ldw(d, d);
    SB();
    return 0u;
  };

  if (idx < 18) {
    gemm_unit<true>(c, wqkv, b1, rh * 9 + (idx >> 1), cq * 2 + (idx & 1), ROT1(((idx >> 1) + 5 * (idx & 1)) % 10), mytag, pre_epi);
  } else if (idx < 31) {
    const int j = idx - 18;
    const int rt = j < 12 ? rh * 7 + (j >> 1) : 6;
    const int ct = cq * 2 + (j < 12 ? (j & 1) : rh);
    gemm_unit<false>(c, w1, b1, rt, ct, ROT1((((j >> 1) * 3 >> 1) + 5 * (j & 1) + 2) % 10), mytag, pre_epi);
  } else {
    StatLd sl;
    const int u = 248 + xcd * 3 + (w < 3 ? w : 0), rcb = repack_cons(c, u);
    stat_issue(c, sl, rcb);
    for (int i = 0; i < 3; ++i) repack_issue(c, 248 + xcd * 3 + i, i * 10240);
    WAITV(0);
    stat_finish(c, sl);
    mytag = next_tag(sl.my);
    BAR();
    for (int i = 0; i < 3; ++i) repack_store(c, 248 + xcd * 3 + i, i * 10240);
    WAITV(0);
    BAR();
    if (w < 3) st32(c.rs, repack_flagoff(u, rcb), next_tag(sl.r2));
    pre_epi(1, pre_epi(0, 0u));
  }

  const int m16 = l & 15, kg = l >> 4;
  {
    bool need;
    const int sl = l & 7, second = sl & 1;
    if (w < 6) {
      const int ct = (w * 4) / 3 + (sl >> 1);
      const bool split = (64 * p) / 158 != (64 * p + 63) / 158;
      need = 192 * ct < 256 * w + 256 && (second == 0 || split);
    } else {
      const int J = (64 * p) & 1023;
      const bool split = J / 114 != (J + 63) / 114;
      need = second == 0 || split;
    }
    need = need && l < 8;
    const int fo = foff + 64 + sl * 4;
    _Pragma("unroll 1") for (unsigned spins = 0; spins < SPIN_CAP; ++spins) {
      const unsigned v = ld32(c.ws, fo);
      if (__all(v == mytag || !need)) break;
      __builtin_amdgcn_s_sleep(1);
    }
    __builtin_amdgcn_fence(__ATOMIC_ACQUIRE, "wavefront");
  }
  STAMP(1, 0);
  STAMPW(1, 4, 448);
  f32x4 acc[4][5];
#pragma unroll
  for (int a = 0; a < 4; ++a)
#pragma unroll
    for (int b = 0; b < 5; ++b) acc[a][b] = f32x4{0.f, 0.f, 0.f, 0.f};
  v4u ra[PA][4];
  auto lda = [&](int d, int k) {
    const int so = aoff + kidx(k) * 4096;
#pragma unroll
    for (int j = 0; j < 4; ++j) ra[d][j] = ld128(c.rs, l * 16 + j * 1024, so);
  };
#pragma unroll
  for (int d = 0; d < PA; ++d) lda(d, d);
  SB();
#pragma unroll
  for (int k = 0; k < 10; ++k) {
    if (k < 8 || w >= 6) {
#pragma unroll
      for (int a = 0; a < 4; ++a)
#pragma unroll
        for (int b = 0; b < 5; ++b) acc[a][b] = mfma(rb[k % PW][b], ra[k % PA][a], acc[a][b]);
    }
    SB();
    if (k + PA < 8 || (k + PA < 10 && w >= 6)) lda(k % PA, k + PA);
    if (k + PW < 8 || (k + PW < 10 && w >= 6)) ldw(k % PW, k + PW);
    SB();
  }
  STAMP(1, 1);
  STAMPW(1, 5, 448);
  const int ert = w >> 1, eb0 = (w & 1) * 3, enb = (w & 1) ? 2 : 3;
  const int row = vrow2row(64 * p + 16 * ert + m16);
  v2u xr[3], br[3];
#pragma unroll
  for (int b = 0; b < 3; ++b) {
    const int col = 80 * cg + 16 * (eb0 + (b < enb ? b : 0)) + 4 * kg;
    xr[b] = *(const v2u*)(x + row * D + col);
    br[b] = *(const v2u*)(b2 + col);
  }
  f32x4* red = (f32x4*)smem;
#pragma unroll
  for (int a = 0; a < 4; ++a)
#pragma unroll
    for (int b = 0; b < 5; ++b) red[((w * 4 + a) * 5 + b) * 64 + l] = acc[a][b];
  WAITL0();
  BAR();
  STAMP(1, 2);
  if (threadIdx.x == 0) st32(c.rs, OFF_R + c.b * 4, mytag);
#pragma unroll
  for (int b = 0; b < 3; ++b) {
    if (b < enb) {
      const f32x4* rp = red + (ert * 5 + eb0 + b) * 64 + l;
      const f32x4 g = ((rp[0] + rp[1280]) + (rp[2560] + rp[3840])) + (rp[5120] + rp[6400]);
      const f32x4 sv = rp[7680] + rp[8960];
      float r[4];
      const float bb[4] = {bflo(br[b].x), bfhi(br[b].x), bflo(br[b].y), bfhi(br[b].y)};
      const float xx[4] = {bflo(xr[b].x), bfhi(xr[b].x), bflo(xr[b].y), bfhi(xr[b].y)};
#pragma unroll
      for (int j = 0; j < 4; ++j) {
        const float gg = g[j] + bb[j];
        const float si = gg * __builtin_amdgcn_rcpf(1.0f + __expf(-gg));
        r[j] = xx[j] + sv[j] * si;
      }
      *(v2u*)(out + row * D + 80 * cg + 16 * (eb0 + b) + 4 * kg) = v2u{pk2(r[0], r[1]), pk2(r[2], r[3])};
    }
  }
  STAMP(1, 3);
}

extern "C" void kernel_launch(void* const* d_in, const int* in_sizes, int n_in, void* d_out, int out_size,
                              void* d_ws, size_t ws_size, hipStream_t stream) {
  const u16* x = (const u16*)d_in[0];
  const u16* wqkv = (const u16*)d_in[1];
  const u16* wo = (const u16*)d_in[2];
  const u16* w1 = (const u16*)d_in[4];
  const u16* b1 = (const u16*)d_in[5];
  const u16* w2 = (const u16*)d_in[6];
  const u16* b2 = (const u16*)d_in[7];
  (void)hipFuncSetAttribute((const void*)fused_kernel, hipFuncAttributeMaxDynamicSharedMemorySize, LDS_BYTES);
  fused_kernel<<<256, 512, LDS_BYTES, stream>>>(x, wqkv, wo, w1, b1, w2, b2, (char*)d_ws, (u16*)d_out);
}
```

```cpp
#include <hip/hip_runtime.h>
#include <hip/hip_bf16.h>
#include <type_traits>

typedef unsigned short u16;
typedef short bf16x8 __attribute__((ext_vector_type(8)));
typedef short bf16x4 __attribute__((ext_vector_type(4)));
typedef float f32x4 __attribute__((ext_vector_type(4)));
typedef unsigned v4u __attribute__((ext_vector_type(4)));
typedef unsigned v2u __attribute__((ext_vector_type(2)));
typedef __bf16 bfx2 __attribute__((ext_vector_type(2)));
typedef float f32x2 __attribute__((ext_vector_type(2)));
typedef __amdgpu_buffer_rsrc_t rsrc_t;
#define LAS __attribute__((address_space(3)))
#define GAS __attribute__((address_space(1)))
#define DEVI __device__ __forceinline__

constexpr int S = 2048, D = 640, F = 1536;
constexpr int OFF_R = 0;
constexpr int OFF_FLAG = 4096;
constexpr int OFF_STAT = OFF_FLAG + 262144;
constexpr int OFF_H2 = OFF_STAT + 327680;
constexpr int OFF_O = OFF_H2 + 6291456;
constexpr int OFF_WP = OFF_O + 2621440;
constexpr int WS_USED = OFF_WP + 2785280;
constexpr int SC1 = 16;
constexpr unsigned SPIN_CAP = 1u << 18;

DEVI float bfhi(unsigned u) { return __uint_as_float(u & 0xFFFF0000u); }
DEVI float bflo(unsigned u) { return __uint_as_float(u << 16); }
DEVI unsigned pk2(float a, float b) {
  f32x2 v = {a, b};
  bfx2 r = __builtin_convertvector(v, bfx2);
  return __builtin_bit_cast(unsigned, r);
}
DEVI float rbf(float a) { return bflo(pk2(a, a)); }
DEVI f32x4 mfma(v4u a, v4u b, f32x4 c) {
  return __builtin_amdgcn_mfma_f32_16x16x32_bf16(__builtin_bit_cast(bf16x8, a), __builtin_bit_cast(bf16x8, b), c, 0, 0, 0);
}
DEVI v2u trread(const char* p) {
  bf16x4 r = __builtin_amdgcn_ds_read_tr16_b64_v4i16((LAS bf16x4*)p);
  return __builtin_bit_cast(v2u, r);
}
DEVI float xsum16_32(float v) {
  auto a = __builtin_amdgcn_permlane16_swap(__float_as_uint(v), __float_as_uint(v), false, false);
  v = __uint_as_float(a[0]) + __uint_as_float(a[1]);
  auto b = __builtin_amdgcn_permlane32_swap(__float_as_uint(v), __float_as_uint(v), false, false);
  return __uint_as_float(b[0]) + __uint_as_float(b[1]);
}
DEVI float xmax16_32(float v) {
  auto a = __builtin_amdgcn_permlane16_swap(__float_as_uint(v), __float_as_uint(v), false, false);
  v = fmaxf(__uint_as_float(a[0]), __uint_as_float(a[1]));
  auto b = __builtin_amdgcn_permlane32_swap(__float_as_uint(v), __float_as_uint(v), false, false);
  return fmaxf(__uint_as_float(b[0]), __uint_as_float(b[1]));
}
template <int CTRL> DEVI float dpp(float x) {
  return __int_as_float(__builtin_amdgcn_mov_dpp(__float_as_int(x), CTRL, 0xf, 0xf, true));
}
DEVI float wsum64(float v) {
  v += dpp<0xB1>(v);
  v += dpp<0x4E>(v);
  v += dpp<0x141>(v);
  v += dpp<0x128>(v);
  return xsum16_32(v);
}
DEVI float vmax(float a, float b) { return __builtin_elementwise_maximum(a, b); }
DEVI float vmax3(float a, float b, float c) { return vmax(vmax(a, b), c); }
template <bool MAX> DEVI float red4(float a, float b, float c, float d) {
  auto s1 = __builtin_amdgcn_permlane16_swap(__float_as_uint(a), __float_as_uint(b), false, false);
  auto s2 = __builtin_amdgcn_permlane16_swap(__float_as_uint(c), __float_as_uint(d), false, false);
  const float x = MAX ? vmax(__uint_as_float(s1[0]), __uint_as_float(s1[1])) : __uint_as_float(s1[0]) + __uint_as_float(s1[1]);
  const float y = MAX ? vmax(__uint_as_float(s2[0]), __uint_as_float(s2[1])) : __uint_as_float(s2[0]) + __uint_as_float(s2[1]);
  auto s3 = __builtin_amdgcn_permlane32_swap(__float_as_uint(x), __float_as_uint(y), false, false);
  return MAX ? vmax(__uint_as_float(s3[0]), __uint_as_float(s3[1])) : __uint_as_float(s3[0]) + __uint_as_float(s3[1]);
}
DEVI void bcast4(float z, float (&o)[4]) {
  auto t = __builtin_amdgcn_permlane32_swap(__float_as_uint(z), __float_as_uint(z), false, false);
  auto u0 = __builtin_amdgcn_permlane16_swap(t[0], t[0], false, false);
  auto u1 = __builtin_amdgcn_permlane16_swap(t[1], t[1], false, false);
  o[0] = __uint_as_float(u0[0]); o[1] = __uint_as_float(u0[1]); o[2] = __uint_as_float(u1[0]); o[3] = __uint_as_float(u1[1]);
}
DEVI float wsum32(float v) {
  v += dpp<0xB1>(v);
  v += dpp<0x4E>(v);
  v += dpp<0x141>(v);
  v += dpp<0x128>(v);
  auto a = __builtin_amdgcn_permlane16_swap(__float_as_uint(v), __float_as_uint(v), false, false);
  return __uint_as_float(a[0]) + __uint_as_float(a[1]);
}
DEVI unsigned next_tag(unsigned r) { return r * 1664525u + 1013904223u; }
#define WAITV(n) asm volatile("s_waitcnt vmcnt(%0)" ::"i"(n) : "memory")
#define WAITL0() __builtin_amdgcn_s_waitcnt(0xC07F)
#define SB() __builtin_amdgcn_sched_barrier(0)
#define BAR() __builtin_amdgcn_s_barrier()
DEVI void glds16(unsigned voff, const void* sbase, unsigned lds_dst) {
  asm volatile("s_mov_b32 m0, %2\n\ts_nop 0\n\tglobal_load_lds_dwordx4 %0, %1" ::"v"(voff), "s"(sbase), "s"(lds_dst) : "memory");
}
DEVI void glds16s(unsigned voff, const void* sbase, unsigned lds_dst) {
  asm volatile("s_mov_b32 m0, %2\n\ts_nop 0\n\tglobal_load_lds_dwordx4 %0, %1 sc1" ::"v"(voff), "s"(sbase), "s"(lds_dst) : "memory");
}
DEVI void glds16m(unsigned voff, const void* sbase, unsigned lds_dst, unsigned long long mask) {
  asm volatile("s_mov_b32 m0, %2\n\ts_mov_b64 exec, %3\n\tglobal_load_lds_dwordx4 %0, %1\n\ts_mov_b64 exec, -1"
               ::"v"(voff), "s"(sbase), "s"(lds_dst), "s"(mask) : "memory");
}
#define PIN(x) asm volatile("" : "+v"(x))
DEVI int vrow2row(int v) { return ((v & 1023) << 1) | (v >> 10); }
#define RLX_AGENT __ATOMIC_RELAXED, __HIP_MEMORY_SCOPE_AGENT
DEVI unsigned ld32(const char* ws, int off) { return __hip_atomic_load((const GAS unsigned*)(ws + off), RLX_AGENT); }
DEVI v2u ld64(const char* ws, int off) {
  const unsigned long long v = __hip_atomic_load((const GAS unsigned long long*)(ws + off), RLX_AGENT);
  return v2u{(unsigned)v, (unsigned)(v >> 32)};
}
#define LDAUX SC1
DEVI v4u ld128(rsrc_t rs, int off, int soff) { return __builtin_amdgcn_raw_buffer_load_b128(rs, off, soff, LDAUX); }
DEVI void st32(rsrc_t rs, int off, unsigned v) { __builtin_amdgcn_raw_buffer_store_b32(v, rs, off, 0, SC1); }
DEVI void st64(rsrc_t rs, int off, v2u v) { __builtin_amdgcn_raw_buffer_store_b64(v, rs, off, 0, SC1); }
DEVI void st128(rsrc_t rs, int off, v4u v) { __builtin_amdgcn_raw_buffer_store_b128(v, rs, off, 0, SC1); }
constexpr int OOB = 0x7FFFFFF0;

DEVI int blk_qkv(int par, int tI, int c) { return ((tI * 2 + (c & 1)) << 3) | ((c >> 1) * 2 + par); }
DEVI int blk_h(int rt, int ct) {
  const int e = ct & 1, rh = rt < 6 ? 0 : (rt > 6 ? 1 : e);
  const int j = rt == 6 ? 12 : (rt - 7 * rh) * 2 + e;
  return ((18 + j) << 3) | ((ct >> 1) * 2 + rh);
}
#define P2MAP 1
DEVI int blk_p2(int p, int c) { return (((p & 7) * 4 + (c & 3)) << 3) | ((p >> 3) * 2 + (c >> 2)); }

#define STAMP(i)
#define STAMP2(i)
#define SDUMP()

#define ROT1(x) (x)
#define RMODE 2
#define ROTQ (((idx >> 1) + 5 * (idx & 1) + cq + 5 * rh) % 10)
#define ROTH ((((j >> 1) * 3 >> 1) + 5 * (j & 1) + 2 + cq + 5 * rh) % 10)
#define HWAIT 4
constexpr int STAGE = 49152;
constexpr int L_RP = 3 * STAGE;
constexpr int LDS_BYTES = 163840;
constexpr int L_CS = LDS_BYTES - 2048;
constexpr int L_FD = L_CS + 1024;
constexpr int L_CT = L_FD + 512;
constexpr int L_IMG = 0;
constexpr int RS = 528;
constexpr int L_WP = 128 * RS;
constexpr int L_PR = L_WP + 81920;
constexpr int L_PB = L_PR + 1280;
typedef volatile LAS unsigned lds_u32;

struct Ctx {
  const u16* x; const u16* w2; const u16* wo; const char* ws;
  char* smem; unsigned lds0; rsrc_t rs; int b, w, l;
};

DEVI void lds_arrive(const Ctx& c, int word) {
  WAITL0();
  if (c.l == 0) __hip_atomic_fetch_add((LAS unsigned*)(LAS char*)(c.smem + L_CT) + word, 1u, __ATOMIC_RELAXED, __HIP_MEMORY_SCOPE_WORKGROUP);
}
DEVI unsigned lds_word(const Ctx& c, int word) { return *((lds_u32*)(LAS char*)(c.smem + L_CT) + word); }
DEVI void lds_await(const Ctx& c, int word, unsigned n, bool nap = false) {
  while (lds_word(c, word) != n) { if (nap) __builtin_amdgcn_s_sleep(1); }
  asm volatile("" ::: "memory");
}

DEVI void repack_issue(const Ctx& c, int lw, int u, unsigned ldsoff) {
  const int ks = u >> 2, qd = u & 3;
  int A, B, C;
  if (ks < 48) { A = 32 * ks; B = 16; C = 4; }
  else {
    const int k2 = ks - 48, j = (k2 * 13) >> 6, lo = k2 - 5 * j;
    B = 20;
    if (lo < 4) { A = 40 * (4 * j + lo); C = 4; } else { A = 160 * j + 16; C = 40; }
  }
  const u16* src = (ks < 48 ? c.w2 : c.wo) + A * D + qd * 160;
#pragma unroll
  for (int i = 0; i < 3; ++i) {
    if (i < 2 || lw < 2) {
      const unsigned e = lw * 64 + c.l + i * 256, r = (e * 3277u) >> 16;
      const unsigned row = (r >> 4) * B + ((r >> 2) & 3) * C + (r & 3);
      glds16(row * 1280u + (e - 20u * r) * 16u, src, c.lds0 + ldsoff + lw * 1024 + i * 4096);
    }
  }
}
DEVI void repack_store(const Ctx& c, int lw, int u, unsigned ldsoff) {
  const int ks = u >> 2, qd = u & 3;
  const char* base = c.smem + ldsoff + (c.l >> 2) * 320 + lw * 32 + (c.l & 3) * 8;
  const int dst = OFF_WP + (qd * 2 * 68 + ks) * 5120 + c.l * 16;
#pragma unroll
  for (int i = 0; i < 3; ++i) {
    if (i < 2 || lw < 2) {
      const int T = lw + 4 * i;
      const v2u a = trread(base + i * 128), b = trread(base + i * 128 + 16 * 320);
      st128(c.rs, dst + (T < 5 ? T * 1024 : 68 * 5120 + (T - 5) * 1024), v4u{a.x, a.y, b.x, b.y});
    }
  }
}
DEVI int repack_cons(const Ctx& c, int u) { return blk_p2(c.l & 31, (u & 3) * 2 + (c.l >> 5)); }
DEVI int repack_flagoff(int u, int cb) {
  const int ks = u >> 2;
  const int cw = ks < 48 ? 4 + ks / 12 : (ks - 48) / 5;
  const int sl = ks < 48 ? ks % 12 : (ks - 48) % 5;
  return OFF_FLAG + (cb * 8 + cw) * 128 + sl * 4;
}

template <typename F> DEVI void row_stats(const Ctx& c, F more_loads) {
  const int l = c.l, hl = l & 31;
  const int par = c.b & 1, J = (((c.b >> 1) & 3) * 32 + (c.b >> 3)) * 8 + c.w * 2 + (l >> 5);
  const char* rp = (const char*)(c.x + (2 * J + par) * D);
  const v4u a = *(const v4u*)(rp + hl * 16), b = *(const v4u*)(rp + 512 + hl * 16);
  const v2u d = *(const v2u*)(rp + 1024 + hl * 8);
  const int v = par * 1024 + J;
  const int tI = (J * 575) >> 16, rem = J - 114 * tI;
  const int hrt = (v * 1660) >> 18;
  int cb, slot;
  bool ok = hl < 24;
  if (hl < 8) { cb = blk_qkv(par, tI, hl); slot = rem + 7; slot += (slot * 265) >> 13; }
  else if (hl < 16) { cb = blk_qkv(par, tI + 1, hl - 8); slot = rem - 107; ok = rem >= 107 && tI < 8; }
  else { cb = blk_h(hrt, hl & 7); slot = v - ((158 * hrt + 15) & ~15); slot = slot < 0 ? slot + 160 : slot; }
  cb = ok ? cb : 0;
  const unsigned r = ld32(c.ws, OFF_R + cb * 4);
  more_loads();
  SB();
  float e[20];
#pragma unroll
  for (int j = 0; j < 4; ++j) { e[2 * j] = bflo(a[j]); e[2 * j + 1] = bfhi(a[j]); e[8 + 2 * j] = bflo(b[j]); e[9 + 2 * j] = bfhi(b[j]); }
  e[16] = bflo(d.x); e[17] = bfhi(d.x); e[18] = bflo(d.y); e[19] = bfhi(d.y);
  float s0 = 0.f, s1 = 0.f;
#pragma unroll
  for (int j = 0; j < 10; ++j) { s0 += e[j]; s1 += e[10 + j]; }
  const float mean = wsum32(s0 + s1) * (1.0f / D);
  s0 = 0.f; s1 = 0.f;
#pragma unroll
  for (int j = 0; j < 10; ++j) { const float d0 = e[j] - mean, d1 = e[10 + j] - mean; s0 = fmaf(d0, d0, s0); s1 = fmaf(d1, d1, s1); }
  const float var = wsum32(s0 + s1) * (1.0f / D);
  const float den = __builtin_amdgcn_sqrtf(rbf(rbf(var) + 1e-5f));
  st64(c.rs, ok ? OFF_STAT + (cb * 160 + slot) * 8 : OOB, v2u{pk2(mean, den), next_tag(r)});
}

template <bool QKV, bool LOADER, typename P2>
DEVI unsigned gemm_unit(const Ctx& c, const u16* __restrict__ W, const u16* __restrict__ b1, int rt, int ct, int rot,
                    unsigned& mytag, P2 p2) {
  constexpr int MI = QKV ? 2 : 5, NI = QKV ? 15 : 6;
  constexpr int BOFF = QKV ? 16384 : 20480;
  constexpr int LDW = QKV ? 3 * D : F;
  constexpr int NPA = QKV ? 4 : 5, NPB = QKV ? 8 : 6, NP = NPA + NPB;
  __builtin_amdgcn_s_waitcnt(0x0F70);
  const int l = c.l, w = c.w;
  char* smem = c.smem;
  const int par = QKV ? (rt >= 9 ? 1 : 0) : 0;
  const int tI = QKV ? rt - 9 * par : 0;
  const int v0 = QKV ? par * 1024 + 114 * tI - 7 : 158 * rt;
  const int vlo = QKV ? par * 1024 : 0, vhi = QKV ? par * 1024 + 1023 : 2047;
  const int vA = (v0 + 15) & ~15, ntail = (v0 + 14) & 15;
  const int orow = QKV ? 127 : 144 + ntail;
  const int m16 = l & 15, kg = l >> 4;

  int apr[3], apv[3], aob[2], aoo0, aoo1;
  unsigned mA0, mA1;
  const int dq = m16 - 4 * kg;
  auto attn_setup = [&]() {
    const int hp = w & 1, q4 = (l >> 2) & 3, pp = l & 3;
    const int P = 2 * ct + hp;
    aoo0 = (P + (P >> 2)) * 4096 + l * 16;
    aoo1 = kg == 0 ? ((P >> 2) * 5 + 4) * 4096 + (P & 3) * 256 + m16 * 16 : OOB - 0x2000000;
    const int r0 = 7 - ((2 * tI) & 15) + 32 * (w >> 1);
    auto clampr = [](int r) { return r < 0 ? 0 : (r > 123 ? 123 : r); };
#pragma unroll
    for (int t = 0; t < 3; ++t) {
      apr[t] = L_IMG + clampr(r0 + 16 * (t - 1) + m16) * RS + hp * 80 + kg * 8;
      apv[t] = L_IMG + clampr(r0 + 16 * (t - 1) + 4 * kg + q4) * RS + 320 + hp * 80 + pp * 8;
      PIN(apr[t]); PIN(apv[t]);
    }
    mA0 = (dq <= -9 ? 0xFFFFu : 0u) | (dq - 1 <= -9 ? 0xFFFF0000u : 0u);
    mA1 = (dq - 2 <= -9 ? 0xFFFFu : 0u) | (dq - 3 <= -9 ? 0xFFFF0000u : 0u);
#pragma unroll
    for (int ui = 0; ui < 2; ++ui) {
      const int rq = r0 + 16 * ui + m16, vt = v0 + r0 + 16 * ui;
      const bool ok = rq >= 7 && rq < 121 && vt + m16 <= vhi;
      aob[ui] = ok ? OFF_O + ((vt >> 6) * 80 + ((vt >> 4) & 3)) * 1024 : 0x1000000;
      PIN(aob[ui]);
    }
    PIN(aoo0); PIN(aoo1); PIN(mA0); PIN(mA1);
  };

  if constexpr (LOADER) {
    const int lw = w - 4;
    unsigned va[NPA], vb[2];
    {
      const int j = l >> 3, pos = l & 7;
      const int asw = (pos ^ (((lw & 1) << 2) | (j >> 1))) << 4;
#pragma unroll
      for (int i = 0; i < NPA; ++i) {
        const int lr = (lw + 4 * i) * 8 + j;
        int v = QKV ? v0 + lr - i : (i < 4 || lr < 144 + ntail ? vA + lr : vA + lr - 160);
        v = v < vlo ? vlo : (v > vhi ? vhi : v);
        va[i] = vrow2row(v) * 1280 + asw;
      }
      if (QKV) {
#pragma unroll
        for (int i = 0; i < 2; ++i) {
          const int f = (l >> 5) | ((lw & 1) << 1) | (i << 2);
          int T = ((l >> 1) & 15) ^ f;
          T = T > 14 ? 14 : T;
          vb[i] = (2 * (lw + 4 * i) + (l >> 5)) * 3840 + ((T * 13) >> 6) * 1120 + T * 32 + (l & 1) * 16;
        }
      } else {
        const int c32 = (pos >> 1) ^ (((j >> 1) & 1) | ((lw & 1) << 1));
        vb[0] = (8 * lw + j) * 3072 + c32 * 32 + (pos & 1) * 16;
        vb[1] = vb[0] + 32 * 3072;
      }
    }
    const unsigned long long omask = lw == ((orow >> 3) & 3) ? ~(0xFFull << (8 * (orow & 7))) : ~0ull;
    const char* Wc = (const char*)(W + ct * (QKV ? 80 : 192));
    auto issue = [&](int s, int buf, int lo, int hi) {
      const int kst = rot + s >= 10 ? rot + s - 10 : rot + s;
      const unsigned lb = c.lds0 + buf * STAGE + lw * 1024;
      const char* sa = (const char*)c.x + kst * 128;
      const char* sw = Wc + kst * (LDW * 128);
#pragma unroll
      for (int n = lo; n < hi; ++n) {
        if (n < 2 * NPA && !(n & 1)) {
          if (!QKV && n / 2 < NPA - 1) glds16(va[n / 2], sa, lb + (n / 2) * 4096);
          else glds16m(va[n / 2], sa, lb + (n / 2) * 4096, omask);
        } else {
          const int i = n < 2 * NPA ? n / 2 : n - NPA;
          glds16(vb[i & 1], sw + (i >> 1) * (QKV ? 16 * 3840 : 128), lb + BOFF + i * 4096);
        }
      }
    };
    auto G = [](int s) constexpr { return s <= 2 ? 6 : (s == 3 ? 8 : (s == 4 ? 10 : NP)); };
    issue(0, 2, 0, NP);
    STAMP(1);
    const int ru = c.b, rcb = repack_cons(c, ru);
    unsigned r2, my, rpf, bv;
    v2u sg;
    asm volatile("global_load_dword %0, %2, %4 sc1\n\tglobal_load_dword %1, %3, %4 sc1"
                 : "=&v"(r2), "=&v"(my) : "v"(rcb * 4), "v"(c.b * 4 + (l & 0)), "s"(c.ws + OFF_R) : "memory");
    issue(1, 0, 0, G(1));
    if (QKV) attn_setup();
    const int plr = lw * 40 + (l < 40 ? l : 0);
    int sgo = (c.b * 160 + plr) * 8, rfo = repack_flagoff(ru, rcb);
    bool need;
    if (QKV) { const int rho = plr - (plr >> 5); need = (plr & 31) != 31 && rho < 121 && v0 + rho >= vlo && v0 + rho <= vhi; }
    else need = (plr < 144 + ntail ? vA + plr : vA + plr - 160) <= vhi && (plr < 144 + ntail || plr >= 146 + ntail);
    PIN(sgo); PIN(rfo);
    STAMP(2);
    WAITV(G(1) + 2);
    BAR();
    STAMP(3);
#pragma unroll
    for (int s = 0; s < 8; ++s) {
      issue(s + 1, s % 3, G(s + 1), NP);
      issue(s + 2, (s + 1) % 3, 0, G(s + 2));
      if (s == 1 && lw == 0) {
        asm volatile("" : "+v"(r2));
        st32(c.rs, rfo, next_tag(r2));
        WAITV(G(s + 2) + 1);
      } else if (s == 5) {
        asm volatile("global_load_dwordx2 %0, %1, %2 sc1" : "=&v"(sg) : "v"(sgo), "s"(c.ws + OFF_STAT) : "memory");
        asm volatile("global_load_dword %0, %1, %2 sc1" : "=&v"(rpf) : "v"((c.b * 8 + (l & 16 ? lw : w)) * 128 + m16 * 4), "s"(c.ws + OFF_FLAG) : "memory");
        if (!QKV) asm volatile("global_load_ushort %0, %1, %2" : "=&v"(bv) : "v"((lw * 48 + (l < 48 ? l : 0)) * 2), "s"(b1 + ct * 192) : "memory");
        WAITV(G(s + 2) + (QKV ? 2 : 3));
      } else WAITV(G(s + 2));
      if (lw == 0) STAMP2(8 + s);
      BAR();
      if (lw != 0) STAMP2(8 + s);
    }
    {
      asm volatile("" : "+v"(my), "+v"(sg), "+v"(rpf));
      mytag = next_tag(my);
      if (!__all(sg.y == mytag || !need)) {
        _Pragma("unroll 1") for (unsigned spins = 0; spins < SPIN_CAP; ++spins) {
          sg = ld64(c.ws, OFF_STAT + sgo);
          if (__all(sg.y == mytag || !need)) break;
        }
      }
      const float inv = need ? __builtin_amdgcn_rcpf(bfhi(sg.x)) : 0.f;
      if (l < 40) *(f32x2*)(smem + L_PR + plr * 8) = f32x2{need ? -bflo(sg.x) * inv : 0.f, inv};
      if (!QKV) { asm volatile("" : "+v"(bv)); if (l < 48) *(float*)(smem + L_PB + (lw * 48 + l) * 4) = bflo(bv); }
    }
    p2(1, rpf);
    p2(20, 0u);
    WAITV(5);
    WAITL0();
    BAR();
    STAMP(4);
    p2(21, 0u); p2(22, 0u); p2(23, 0u);
    if (QKV) { p2(24, 0u); p2(25, 0u); }
    lds_await(c, QKV ? 1 : 0, 4u, true);
    if (!QKV) { p2(24, 0u); p2(25, 0u); }
    p2(13, 0u);
    STAMP(5);
  } else {
    unsigned my, fr;
    int fdst;
    row_stats(c, [&]() {
      my = ld32(c.ws, OFF_R + c.b * 4);
      const int vfirst = QKV ? v0 + 7 : v0, vlast = (vfirst + (QKV ? 113 : 157)) > vhi ? vhi : vfirst + (QKV ? 113 : 157);
      const int p = (vfirst >> 6) + ((l >> 3) & 3), cc = l & 7;
      const int cb = blk_p2(p & 31, cc);
      const int second = p * 64 >= vfirst ? 0 : 1;
      const bool ok = p <= (vlast >> 6) && l < 32;
      fdst = ok ? OFF_FLAG + (cb * 8 + (QKV ? 0 : 4) + (ct >> 1)) * 128 + (16 + (ct & 1) * 2 + second) * 4 : OOB;
      fr = ld32(c.ws, OFF_R + cb * 4);
      repack_issue(c, w, c.b, L_RP);
    });
    mytag = next_tag(my);
    if (w == 0) *(v2u*)(smem + L_FD + l * 8) = v2u{(unsigned)fdst, next_tag(fr)};
    if (w == 1 && l < 8) *(unsigned*)(smem + L_CT + l * 4) = 0u;
    if ((QKV || w == 3) && l < 24) *(v4u*)(smem + (l >> 3) * STAGE + (QKV ? 32 * w + 31 : orow) * 128 + (l & 7) * 16) = v4u{0x3F803F80u, 0x3F803F80u, 0x3F803F80u, 0x3F803F80u};
    const int wm = QKV ? w : (w >> 1), wn = QKV ? 0 : (w & 1);
    const int fsw = m16 >> 1;
    const int xrow = (wm * (MI * 16) + m16) * 128;
    int xa[2] = {xrow + ((kg ^ fsw) << 4), xrow + (((4 + kg) ^ fsw) << 4)};
    constexpr int NWB = QKV ? 8 : 6;
    int wb[NWB];
    {
      const int q = (l >> 2) & 3, pp = l & 3;
      if (QKV) {
        const int f = q | ((kg & 1) << 2);
#pragma unroll
        for (int i = 0; i < NWB; ++i) wb[i] = BOFF + (8 * kg + q) * 512 + pp * 8 + ((i ^ f) << 5);
      } else {
        const int swz = ((q >> 1) & 1) | ((kg & 1) << 1);
#pragma unroll
        for (int i = 0; i < NWB; ++i) {
          const int T = wn * 6 + i;
          wb[i] = BOFF + ((T >> 2) * 8 + kg) * 1024 + q * 128 + (((T & 3) ^ swz) << 5) + pp * 8;
        }
      }
    }
    constexpr int BAD = OOB - 16384;
    int so[MI];
#pragma unroll
    for (int mi = 0; mi < MI; ++mi) {
      const int lr = wm * (MI * 16) + mi * 16 + m16;
      if (!QKV) {
        const int v = lr < 144 + ntail ? vA + lr : vA + lr - 160;
        const bool ok = v <= vhi && (lr < 144 + ntail || lr >= 146 + ntail);
        so[mi] = ok ? OFF_H2 + (((v >> 6) * 48 + ct * 6 + wn * 3) * 4 + ((v >> 4) & 3)) * 1024 + l * 16 : BAD;
        PIN(so[mi]);
      }
    }
    f32x4 acc[MI][NI];
#pragma unroll
    for (int a = 0; a < MI; ++a)
#pragma unroll
      for (int b = 0; b < NI; ++b) acc[a][b] = f32x4{0.f, 0.f, 0.f, 0.f};
    v4u xf[2][MI];
    v2u wf[2 * NI][2];
    constexpr int LAG = 10;
    constexpr int NRD = 2 * (MI + 2 * NI);
#pragma unroll
    for (int a = 0; a < MI; ++a) xf[1][a] = v4u{0u, 0u, 0u, 0u};
#pragma unroll
    for (int b = 2 * NI - LAG / MI; b < 2 * NI; ++b) { wf[b][0] = v2u{0u, 0u}; wf[b][1] = v2u{0u, 0u}; }

    auto rd1 = [&](int r, const char* sb) {
      const int kk = r / (NRD / 2), i = r % (NRD / 2);
      if (i >= 2 && i < 2 + MI) xf[kk][i - 2] = *(const v4u*)(sb + xa[kk] + (i - 2) * 2048);
      else {
        const int j = i < 2 ? i : i - MI, ni = j >> 1, t = j & 1;
        wf[kk * NI + ni][t] = trread(sb + (QKV ? wb[ni & 7] + (ni >> 3) * 256 + kk * 16384 + t * 2048 : wb[ni] + kk * 4096 + t * 512));
      }
    };
    auto mm1 = [&](int m) {
      const int n = m / MI, mi = m % MI, kk = n / NI, ni = n % NI;
      acc[mi][ni] = mfma(v4u{wf[n][0].x, wf[n][0].y, wf[n][1].x, wf[n][1].y}, xf[kk][mi], acc[mi][ni]);
    };
    auto rfirst = [](int m) constexpr {
      if (QKV) return m >= 54 ? NRD : (m * NRD + 53) / 54;
      constexpr signed char T[61] = {0, 1, 2, 3, 4, 5, 6, 7, 8, 9, 9, 9, 10, 11, 11, 11, 11, 12, 13, 13, 13, 13, 14, 15, 15, 15, 15, 16, 17, 17, 18, 19, 20, 21, 22, 23, 24, 24, 25, 26, 26, 26, 27, 28, 28, 28, 29, 30, 30, 31, 32, 32, 33, 34, 34, 34, 34, 34, 34, 34, 34};
      return (int)T[m];
    };
    auto stage = [&](const char* sb) {
#pragma unroll
      for (int m = 0; m < 60; ++m) {
        mm1(m < LAG ? 60 - LAG + m : m - LAG);
        if (rfirst(m) < rfirst(m + 1)) rd1(rfirst(m), sb);
        if (rfirst(m) + 1 < rfirst(m + 1)) rd1(rfirst(m) + 1, sb);
        SB();
      }
    };
    if (QKV) attn_setup();
#pragma unroll
    for (int a = 0; a < MI; ++a) {
      PIN(xf[1][a]);
#pragma unroll
      for (int b = 0; b < NI; ++b) PIN(acc[a][b]);
    }
#pragma unroll
    for (int b = 2 * NI - LAG / MI; b < 2 * NI; ++b) { PIN(wf[b][0]); PIN(wf[b][1]); }
#pragma unroll
    for (int i = 0; i < NWB; ++i) PIN(wb[i]);
    PIN(xa[0]); PIN(xa[1]);
    STAMP(2);
    WAITV(1);
    WAITL0();
    BAR();
    STAMP(3);
    repack_store(c, w, c.b, L_RP);
    int buf = 2;
    int nloop = 9;
#pragma unroll 1
    for (int s = 0; s < nloop; ++s) {
      stage(smem + buf * STAGE);
      buf = buf == 2 ? 0 : buf + 1;
      __builtin_amdgcn_s_waitcnt(0x0070);
      BAR();
      SB();
    }
    STAMP(4);
    stage(smem + 2 * STAGE);
    float* csl = (float*)(smem + L_CS);
    const bool cw = !QKV && wm == 1 && m16 == (orow & 15);
    f32x2 pr[MI];
    f32x4 bs[NI], cs[NI];
    if (!QKV) {
      mm1(54); mm1(59);
      if (cw) {
#pragma unroll
        for (int ni = 0; ni < 4; ++ni) *(f32x4*)(csl + (wn * NI + ni) * 16 + kg * 4) = acc[MI - 1][ni];
      }
      SB();
      mm1(50); mm1(51);
      SB();
      if (cw) {
#pragma unroll
        for (int ni = 4; ni < NI; ++ni) *(f32x4*)(csl + (wn * NI + ni) * 16 + kg * 4) = acc[MI - 1][ni];
      }
    }
    lds_arrive(c, 1);
    SB();
    mm1(52); mm1(53);
    SB();
#pragma unroll
    for (int mi = 0; mi < MI; ++mi) pr[mi] = *(const f32x2*)(smem + L_PR + (wm * (MI * 16) + mi * 16 + m16) * 8);
    if (!QKV) {
#pragma unroll
      for (int ni = 0; ni < NI; ++ni) bs[ni] = *(const f32x4*)(smem + L_PB + (wn * 96 + ni * 16 + kg * 4) * 4);
    }
    SB();
#pragma unroll
    for (int m = 60 - LAG; m < 60; ++m)
      if (m != 52 && m != 53 && (QKV || (m != 54 && m != 59 && m != 50 && m != 51))) mm1(m);
    STAMP(5);
    if (!QKV) {
      lds_await(c, 1, 4u);
      STAMP(14);
#pragma unroll
      for (int ni = 0; ni < NI; ++ni) cs[ni] = *(const f32x4*)(csl + (wn * NI + ni) * 16 + kg * 4);
      SB();
    }
    auto lo2 = [](f32x4 v) { return f32x2{v.x, v.y}; };
    auto hi2 = [](f32x4 v) { return f32x2{v.z, v.w}; };
    if (!QKV) {
      v4u hp[MI][3];
#pragma unroll
      for (int mi = 0; mi < MI; ++mi) {
        const f32x2 c0 = {pr[mi].x, pr[mi].x}, iv = {pr[mi].y, pr[mi].y};
        f32x2 t[NI][2];
#pragma unroll
        for (int ni = 0; ni < NI; ++ni) {
          t[ni][0] = __builtin_elementwise_fma(c0, lo2(cs[ni]), lo2(bs[ni]));
          t[ni][1] = __builtin_elementwise_fma(c0, hi2(cs[ni]), hi2(bs[ni]));
        }
        SB();
#pragma unroll
        for (int ni = 0; ni < NI; ++ni) {
          t[ni][0] = __builtin_elementwise_fma(lo2(acc[mi][ni]), iv, t[ni][0]);
          t[ni][1] = __builtin_elementwise_fma(hi2(acc[mi][ni]), iv, t[ni][1]);
        }
        SB();
#pragma unroll
        for (int ni = 0; ni < NI; ++ni)
#pragma unroll
          for (int e = 0; e < 2; ++e) { t[ni][e].x = fmaxf(t[ni][e].x, 0.f); t[ni][e].y = fmaxf(t[ni][e].y, 0.f); }
        SB();
#pragma unroll
        for (int ni = 0; ni < NI; ++ni) { t[ni][0] *= t[ni][0]; t[ni][1] *= t[ni][1]; }
        SB();
#pragma unroll
        for (int i = 0; i < 3; ++i)
          hp[mi][i] = v4u{pk2(t[2 * i][0].x, t[2 * i][0].y), pk2(t[2 * i][1].x, t[2 * i][1].y),
                          pk2(t[2 * i + 1][0].x, t[2 * i + 1][0].y), pk2(t[2 * i + 1][1].x, t[2 * i + 1][1].y)};
        SB();
        if (mi > 0) asm volatile("" :: "v"(hp[mi - 1][0]), "v"(hp[mi - 1][1]), "v"(hp[mi - 1][2]));
#pragma unroll
        for (int i = 0; i < 3; ++i) st128(c.rs, so[mi] + i * 4096, hp[mi][i]);
        SB();
      }
    } else {
      char* ip = smem + L_IMG + (wm * 31 + m16) * RS + kg * 8;
      char* ip1 = m16 == 15 ? smem + L_IMG + 127 * RS + kg * 8 : ip + 16 * RS;
      v2u pk[NI][2];
      f32x2 t[NI][2][2];
#pragma unroll
      for (int n = 0; n < NI + 4; ++n) {
        if (n < NI) {
#pragma unroll
          for (int r = 0; r < 4; ++r) cs[n][r] = dpp<0x15F>(acc[1][n][r]);
        }
        if (n >= 1 && n - 1 < NI) {
#pragma unroll
          for (int mi = 0; mi < 2; ++mi) {
            const f32x2 c0 = {pr[mi].x, pr[mi].x};
            t[n - 1][mi][0] = c0 * lo2(cs[n - 1]); t[n - 1][mi][1] = c0 * hi2(cs[n - 1]);
          }
        }
        SB();
        if (n >= 4) *(v2u*)(ip + (n - 4) * 32) = pk[n - 4][0];
        SB();
        if (n >= 2 && n - 2 < NI) {
#pragma unroll
          for (int mi = 0; mi < 2; ++mi) {
            const f32x2 iv = {pr[mi].y, pr[mi].y};
            t[n - 2][mi][0] = __builtin_elementwise_fma(lo2(acc[mi][n - 2]), iv, t[n - 2][mi][0]);
            t[n - 2][mi][1] = __builtin_elementwise_fma(hi2(acc[mi][n - 2]), iv, t[n - 2][mi][1]);
          }
        }
        if (n >= 3 && n - 3 < NI) {
#pragma unroll
          for (int mi = 0; mi < 2; ++mi) pk[n - 3][mi] = v2u{pk2(t[n - 3][mi][0].x, t[n - 3][mi][0].y), pk2(t[n - 3][mi][1].x, t[n - 3][mi][1].y)};
        }
        SB();
        if (n >= 4) *(v2u*)(ip1 + (n - 4) * 32) = pk[n - 4][1];
        if (n >= 6) asm volatile("" :: "v"(pk[n - 6][0]), "v"(pk[n - 6][1]));
        SB();
      }
      WAITL0();
      STAMP(1);
    }
  }

  if (QKV) {
    const float sc = 0.22360679774997896f * 1.4426950408889634f;
    const f32x4 z = {0.f, 0.f, 0.f, 0.f};
    const bool noA = tI == 0 && w < 2;
    bool selA[4], val[2][4];
#pragma unroll
    for (int r = 0; r < 4; ++r) {
      selA[r] = dq - r <= -9;
      val[1][r] = selA[r] || (unsigned)(dq - r) <= 7u;
      val[0][r] = (selA[r] && !noA) || (unsigned)(dq - r) <= 7u;
    }
    SB();
    STAMP(12);
    BAR();
    STAMP(13);
    v4u kf[3][2], qf[2][2];
    v2u vf[2][2][2][2];
    v2u kq[10][2];
    {
      const unsigned a0 = c.lds0 + apr[0], a1 = c.lds0 + apr[1], a2 = c.lds0 + apr[2];
#define RD2(i, ad, off) asm volatile("ds_read_b64 %0, %2 offset:%3\n\tds_read_b64 %1, %2 offset:%4" : "=&v"(kq[i][0]), "=&v"(kq[i][1]) : "v"(ad), "i"(off), "i"(off + 32) : "memory")
      RD2(0, a1, 0); RD2(1, a0, 160); RD2(2, a1, 160); RD2(3, a2, 0); RD2(4, a2, 160);
      RD2(5, a1, 40); RD2(6, a0, 200); RD2(7, a1, 200); RD2(8, a2, 40); RD2(9, a2, 200);
#undef RD2
    }
    {
      int m1 = apv[1];
      PIN(m1);
#pragma unroll
      for (int hh = 0; hh < 2; ++hh)
#pragma unroll
        for (int dt = 0; dt < 2; ++dt) {
          vf[0][hh][dt][0] = trread(smem + apv[0] + hh * 40 + dt * 32);
          vf[0][hh][dt][1] = trread(smem + apv[1] + hh * 40 + dt * 32);
          vf[1][hh][dt][0] = trread(smem + m1 + hh * 40 + dt * 32);
          vf[1][hh][dt][1] = trread(smem + apv[2] + hh * 40 + dt * 32);
        }
    }
    SB();
    asm volatile("s_waitcnt lgkmcnt(14)" : "+v"(kq[0][0]), "+v"(kq[0][1]), "+v"(kq[1][0]), "+v"(kq[1][1]), "+v"(kq[2][0]), "+v"(kq[2][1]),
                 "+v"(kq[3][0]), "+v"(kq[3][1]), "+v"(kq[4][0]), "+v"(kq[4][1]) :: "memory");
    asm volatile("" : "+v"(kq[5][0]), "+v"(kq[5][1]), "+v"(kq[6][0]), "+v"(kq[6][1]), "+v"(kq[7][0]), "+v"(kq[7][1]),
                 "+v"(kq[8][0]), "+v"(kq[8][1]), "+v"(kq[9][0]), "+v"(kq[9][1]) :: "memory");
    SB();
#pragma unroll
    for (int hh = 0; hh < 2; ++hh) {
      auto cat = [&](int i) { return v4u{kq[i][0].x, kq[i][0].y, kq[i][1].x, kq[i][1].y}; };
      auto catq = [&](int i) { return v4u{kq[i][0].x, kq[i][0].y, kg == 0 ? kq[i][1].x : 0u, kg == 0 ? kq[i][1].y : 0u}; };
      qf[0][hh] = catq(5 * hh); kf[0][hh] = cat(5 * hh + 1); kf[1][hh] = cat(5 * hh + 2); qf[1][hh] = catq(5 * hh + 3); kf[2][hh] = cat(5 * hh + 4);
    }
    f32x4 sa[4], sb_[4];
#pragma unroll
    for (int un = 0; un < 4; ++un) {
      const int ui = un & 1, hh = un >> 1;
      sa[un] = mfma(kf[ui][hh], qf[ui][hh], z);
      sb_[un] = mfma(kf[ui + 1][hh], qf[ui][hh], z);
    }
    SB();
    float e[4][4], mx[4], sm[4];
#pragma unroll
    for (int un = 0; un < 4; ++un) {
      const int ui = un & 1;
#pragma unroll
      for (int r = 0; r < 4; ++r) { const float t = selA[r] ? sa[un][r] : sb_[un][r]; e[un][r] = val[ui][r] ? t : -3.0e38f; }
      mx[un] = vmax(vmax3(e[un][0], e[un][1], e[un][2]), e[un][3]);
    }
    bcast4(red4<true>(mx[0], mx[1], mx[2], mx[3]) * -sc, mx);
#pragma unroll
    for (int un = 0; un < 4; ++un) {
#pragma unroll
      for (int r = 0; r < 4; ++r) e[un][r] = __builtin_amdgcn_exp2f(fmaf(e[un][r], sc, mx[un]));
      sm[un] = (e[un][0] + e[un][1]) + (e[un][2] + e[un][3]);
    }
    bcast4(__builtin_amdgcn_rcpf(red4<false>(sm[0], sm[1], sm[2], sm[3])), sm);
    v2u om[2][2], ol[2][2];
    v4u pf[4];
    f32x4 o0[4], o1[4];
#pragma unroll
    for (int un = 0; un < 4; ++un) {
      const unsigned p0 = pk2(e[un][0] * sm[un], e[un][1] * sm[un]), p1 = pk2(e[un][2] * sm[un], e[un][3] * sm[un]);
      pf[un] = v4u{p0 & mA0, p1 & mA1, p0 & ~mA0, p1 & ~mA1};
    }
    SB();
#pragma unroll
    for (int un = 0; un < 4; ++un) {
      const int ui = un & 1, hh = un >> 1;
      o0[un] = mfma(v4u{vf[ui][hh][0][0].x, vf[ui][hh][0][0].y, vf[ui][hh][0][1].x, vf[ui][hh][0][1].y}, pf[un], z);
      o1[un] = mfma(v4u{vf[ui][hh][1][0].x, vf[ui][hh][1][0].y, vf[ui][hh][1][1].x, vf[ui][hh][1][1].y}, pf[un], z);
    }
    SB();
#pragma unroll
    for (int un = 0; un < 4; ++un) {
      const int ui = un & 1, hh = un >> 1;
      om[ui][hh] = v2u{pk2(o0[un][0], o0[un][1]), pk2(o0[un][2], o0[un][3])};
      ol[ui][hh] = v2u{pk2(o1[un][0], o1[un][1]), pk2(o1[un][2], o1[un][3])};
    }
#pragma unroll
    for (int ui = 0; ui < 2; ++ui) {
      st128(c.rs, aob[ui] + aoo0, v4u{om[ui][0].x, om[ui][0].y, om[ui][1].x, om[ui][1].y});
      st128(c.rs, aob[ui] + aoo1, v4u{ol[ui][0].x, ol[ui][0].y, ol[ui][1].x, ol[ui][1].y});
    }
  }
  STAMP(6);
  SB();
  unsigned av = 0;
  if (LOADER) av = p2(2, 0u);
  if (QKV || !LOADER) {
    if (LOADER) WAITV(0);
    else { p2(20, 0u); WAITV(5); }
    unsigned old = 0;
    if (l == 0) old = __hip_atomic_fetch_add((LAS unsigned*)(LAS char*)(smem + L_CT), 1u, __ATOMIC_RELAXED, __HIP_MEMORY_SCOPE_WORKGROUP);
    if (__builtin_amdgcn_readfirstlane(old) == (QKV ? 7u : 3u)) {
      const v2u f = *(const v2u*)(smem + L_FD + l * 8);
      st32(c.rs, (int)f.x, f.y);
    }
  }
  if (!LOADER) { p2(21, 0u); p2(22, 0u); p2(23, 0u); p2(24, 0u); }
  STAMP(7);
  return av;
}

__global__ __launch_bounds__(512, 2) void fused_kernel(const u16* __restrict__ x, const u16* __restrict__ wqkv,
                                                       const u16* __restrict__ wo, const u16* __restrict__ w1,
                                                       const u16* __restrict__ b1, const u16* __restrict__ w2,
                                                       const u16* __restrict__ b2, char* __restrict__ ws,
                                                       u16* __restrict__ out) {
  extern __shared__ __attribute__((aligned(16))) char smem[];
  Ctx c;
  c.x = x; c.w2 = w2; c.wo = wo; c.smem = smem; c.ws = ws;
  c.lds0 = (unsigned)(size_t)(LAS char*)smem;
  c.rs = __builtin_amdgcn_make_buffer_rsrc(ws, 0, WS_USED, 0x00020000);
  c.b = blockIdx.x;
  c.l = threadIdx.x & 63;
  c.w = __builtin_amdgcn_readfirstlane(threadIdx.x >> 6);
  const int l = c.l, w = c.w;
  const int xcd = c.b & 7, idx = c.b >> 3;
  const int rh = xcd & 1, cq = xcd >> 1;
  STAMP(0);
  unsigned mytag;

  const int cg = (xcd & 1) * 4 + (idx & 3), p = (xcd >> 1) * 8 + (idx >> 2);
  const int m16 = l & 15, kg = l >> 4;
  const unsigned narr = idx < 18 ? 8u : (idx < 31 ? 4u : 0u);
  f32x4 acc[4][5];

  auto role = [&](auto gw_) {
    constexpr bool gw = decltype(gw_)::value;
    constexpr int nk = gw ? 12 : 5;
    const int woff = OFF_WP + (cg * 68 + (gw ? 12 * (w - 4) : 48 + 5 * w)) * 5120;
    const int aoff = gw ? OFF_H2 + (p * 48 + 12 * (w - 4)) * 4096 : OFF_O + (p * 20 + 5 * w) * 4096;
    const int foff = OFF_FLAG + (c.b * 8 + w) * 128;
    v4u rb[nk][5];
    auto ldw = [&](int k) {
#pragma unroll
      for (int j = 0; j < 5; ++j) rb[k][j] = ld128(c.rs, l * 16 + j * 1024, woff + k * 5120);
    };
    auto p2 = [&](int step, unsigned v) -> unsigned {
      const int fo = foff - (l & 16 ? 512 : 0) + (l & 15) * 4;
      const bool skip = (l & 15) >= (l & 16 ? 5 : 12);
      if (step == 0) return ld32(c.ws, fo);
      if (step == 2) return ld32(c.ws, foff + 64 + (l & 3) * 4);
      if (step == 1) {
        if (!__all(v == mytag || skip)) {
          _Pragma("unroll 1") for (unsigned spins = 0; spins < SPIN_CAP; ++spins) {
            v = ld32(c.ws, fo);
            if (__all(v == mytag || skip)) break;
          }
        }
        __builtin_amdgcn_fence(__ATOMIC_ACQUIRE, "wavefront");
      } else {
        if (step == 10) { ldw(0); ldw(1); }
        if (step == 11) { ldw(2); ldw(3); ldw(4); ldw(gw ? 5 : 0); }
        if (step >= 20) ldw(step - 20);
        if (step == 13) {
#pragma unroll
          for (int j = 0; j < 20; ++j) glds16s(l * 16, c.ws + woff + 6 * 5120 + j * 1024, c.lds0 + L_WP + (w - 4) * 20480 + j * 1024);
        }
        SB();
      }
      return 0u;
    };

    unsigned av = 0;
    if (idx < 18) {
      av = gemm_unit<true, gw>(c, wqkv, b1, rh * 9 + (idx >> 1), cq * 2 + (idx & 1), ROTQ, mytag, p2);
    } else if (idx < 31) {
      const int j = idx - 18;
      const int rt = j < 12 ? rh * 7 + (j >> 1) : 6;
      const int ct = cq * 2 + (j < 12 ? (j & 1) : rh);
      av = gemm_unit<false, gw>(c, w1, b1, rt, ct, ROTH, mytag, p2);
    } else {
      mytag = next_tag(ld32(c.ws, OFF_R + c.b * 4));
      if (!gw) row_stats(c, []() {});
      else {
        for (int i = 0; i < 3; ++i) repack_issue(c, w - 4, 248 + xcd * 3 + i, i * 10240);
      }
      if (threadIdx.x < 8) *(unsigned*)(smem + L_CT + threadIdx.x * 4) = 0u;
      WAITV(0);
      WAITL0();
      BAR();
      if (gw) {
        for (int i = 0; i < 3; ++i) repack_store(c, w - 4, 248 + xcd * 3 + i, i * 10240);
      }
      WAITV(0);
      BAR();
      if (!gw && w < 3) {
        const int u = 248 + xcd * 3 + w, rcb = repack_cons(c, u);
        st32(c.rs, repack_flagoff(u, rcb), next_tag(ld32(c.ws, OFF_R + rcb * 4)));
      }
      if (gw) p2(1, p2(0, 0u));
      BAR();
      if (gw) { p2(10, 0u); p2(11, 0u); p2(13, 0u); }
      else { p2(20, 0u); p2(21, 0u); p2(22, 0u); p2(23, 0u); p2(24, 0u); }
    }

    auto wait_act = [&]() {
      const int J = (64 * p) & 1023;
      const bool split = gw ? (64 * p) / 158 != (64 * p + 63) / 158 : J / 114 != (J + 63) / 114;
      const bool need = (l & 1) == 0 || split;
      const int fo = foff + 64 + (l & 3) * 4;
      if (!gw || idx >= 31 || !__all(av == mytag || !need)) {
        _Pragma("unroll 1") for (unsigned spins = 0; spins < SPIN_CAP; ++spins) {
          const unsigned v = ld32(c.ws, fo);
          if (__all(v == mytag || !need)) break;
          __builtin_amdgcn_s_sleep(2);
        }
      }
      __builtin_amdgcn_fence(__ATOMIC_ACQUIRE, "wavefront");
    };
    v4u ra[nk][4];
    auto lda = [&](int k) {
#pragma unroll
      for (int j = 0; j < 4; ++j) ra[k][j] = ld128(c.rs, l * 16 + j * 1024, aoff + k * 4096);
    };
    auto mm2 = [&](int k) {
#pragma unroll
      for (int a = 0; a < 4; ++a)
#pragma unroll
        for (int b = 0; b < 5; ++b) acc[a][b] = mfma(rb[k][b], ra[k][a], k == 0 ? f32x4{0.f, 0.f, 0.f, 0.f} : acc[a][b]);
      SB();
    };
    auto s3off = [](int t) { return t < 6 ? 61440 + t * 1024 : L_PR + (t < 15 ? t - 6 : t - 7) * 1024; };
    constexpr int eb0 = gw ? 3 : 0, enb = gw ? 2 : 3;
    const int ert = w & 3;
    const int row = vrow2row(64 * p + 16 * ert + m16);
    v2u xr[enb], br[enb];
    auto ldx = [&]() {
#pragma unroll
      for (int b = 0; b < enb; ++b) {
        xr[b] = *(const v2u*)(x + row * D + 80 * cg + 16 * (eb0 + b) + 4 * kg);
        br[b] = *(const v2u*)(b2 + 80 * cg + 16 * (eb0 + b) + 4 * kg);
      }
    };
    f32x4 sv[enb];
    if constexpr (gw) {
      WAITV(0);
      wait_act();
      STAMP(8);
      lda(0); lda(1);
      SB();
      char* wl = smem + L_WP + (w - 4) * 20480 + l * 16;
      auto side = [&](int k, int i) {

        auto la = [&](int s, int j) { ra[s][j] = ld128(c.rs, l * 16 + j * 1024, aoff + s * 4096); };
        auto lw_ = [&](int s, int j) { rb[s][j] = ld128(c.rs, l * 16 + j * 1024, woff + s * 5120); };
        auto ll = [&](int s, int j) { rb[s][j] = *(const v4u*)(wl + (s - 6) * 5120 + j * 1024); };
        if (k == 0) { if (i < 4) la(2, i); }
        else if (k <= 3) { if (i < 8) la(2 * k + 1 + (i >> 2), i & 3); }
        else if (k == 4) { if (i < 4) la(9, i); else if (i < 9) lw_(10, i - 4); }
        else if (k == 5) { if (i < 5) ll(6, i); else if (i < 9) la(10, i - 5); }
        else if (k == 6) { if (i < 5) ll(7, i); else if (i < 10) lw_(11, i - 5); }
        else if (k == 7) { if (i < 5) ll(8, i); else if (i < 9) la(11, i - 5); }
        else if (k == 8) { if (i < 5) ll(9, i); }
        else if (k == 9) { if (i == 0) ldx(); }
      };
#pragma unroll
      for (int k = 0; k < 12; ++k) {
#pragma unroll
        for (int m = 0; m < 20; ++m) {
          acc[m / 5][m % 5] = mfma(rb[k][m % 5], ra[k][m / 5], k == 0 ? f32x4{0.f, 0.f, 0.f, 0.f} : acc[m / 5][m % 5]);
          if (m & 1) { side(k, m >> 1); SB(); }
        }
      }
      STAMP(9);
#pragma unroll
      for (int t = 0; t < 20; ++t) *(f32x4*)(wl + t * 1024) = acc[t / 5][t % 5];
      lds_arrive(c, 2);
      lds_await(c, 4, 4u, true);
#pragma unroll
      for (int b = 0; b < enb; ++b) sv[b] = *(const f32x4*)(smem + (ert * 5 + eb0 + b) * 1024 + l * 16);
    } else {
      wait_act();
      STAMP(8);
      lda(0); lda(1); lda(2); lda(3);
      SB();
#pragma unroll
      for (int k = 0; k < 5; ++k) {
        mm2(k);
        if (k == 0) lda(4);
        if (k == 2) ldx();
        SB();
      }
      STAMP(9);
      lds_await(c, 0, narr);
      if (w != 3) {
#pragma unroll
        for (int t = 0; t < 20; ++t) *(f32x4*)(smem + w * 20480 + t * 1024 + l * 16) = acc[t / 5][t % 5];
      } else {
#pragma unroll
        for (int t = 0; t < 20; ++t)
          if (t != 15) *(f32x4*)(smem + s3off(t) + l * 16) = acc[t / 5][t % 5];
      }
      lds_arrive(c, 3);
      lds_await(c, 3, 4u, true);
#pragma unroll
      for (int b = 4; b >= 0; --b) {
        char* rp = smem + (ert * 5 + b) * 1024 + l * 16;
        f32x4 s3 = *(const f32x4*)(smem + s3off(ert * 5 + b) + l * 16);
        if (b == 0 && w == 3) s3 = acc[3][0];
        const f32x4 t = (*(const f32x4*)rp + *(const f32x4*)(rp + 20480)) + (*(const f32x4*)(rp + 40960) + s3);
        if (b < 3) sv[b] = t;
        else *(f32x4*)rp = t;
        if (b == 3) lds_arrive(c, 4);
      }
    }
    STAMP(10);
    lds_await(c, 2, 4u, !gw);
    STAMP(14);
    if (w == 4 && l == 0) st32(c.rs, OFF_R + c.b * 4, mytag);
#pragma unroll
    for (int b = 0; b < enb; ++b) {
      const char* rp = smem + L_WP + (ert * 5 + eb0 + b) * 1024 + l * 16;
      const f32x4 g = (*(const f32x4*)rp + *(const f32x4*)(rp + 20480)) + (*(const f32x4*)(rp + 40960) + *(const f32x4*)(rp + 61440));
      const float bb[4] = {bflo(br[b].x), bfhi(br[b].x), bflo(br[b].y), bfhi(br[b].y)};
      const float xx[4] = {bflo(xr[b].x), bfhi(xr[b].x), bflo(xr[b].y), bfhi(xr[b].y)};
      float r[4];
#pragma unroll
      for (int j = 0; j < 4; ++j) {
        const float gg = g[j] + bb[j];
        r[j] = fmaf(sv[b][j], gg * __builtin_amdgcn_rcpf(1.0f + __expf(-gg)), xx[j]);
      }
      *(v2u*)(out + row * D + 80 * cg + 16 * (eb0 + b) + 4 * kg) = v2u{pk2(r[0], r[1]), pk2(r[2], r[3])};
    }
    STAMP(11);
  };
  if (w >= 4) role(std::true_type{});
  else role(std::false_type{});
  SDUMP();
}

extern "C" void kernel_launch(void* const* d_in, const int* in_sizes, int n_in, void* d_out, int out_size,
                              void* d_ws, size_t ws_size, hipStream_t stream) {
  const u16* x = (const u16*)d_in[0];
  const u16* wqkv = (const u16*)d_in[1];
  const u16* wo = (const u16*)d_in[2];
  const u16* w1 = (const u16*)d_in[4];
  const u16* b1 = (const u16*)d_in[5];
  const u16* w2 = (const u16*)d_in[6];
  const u16* b2 = (const u16*)d_in[7];
  (void)hipFuncSetAttribute((const void*)fused_kernel, hipFuncAttributeMaxDynamicSharedMemorySize, LDS_BYTES);
  fused_kernel<<<256, 512, LDS_BYTES, stream>>>(x, wqkv, wo, w1, b1, w2, b2, (char*)d_ws, (u16*)d_out);
}
```
